```python
import torch

def _softmax(x, axis=-1):
    return torch.softmax(x, dim=axis)
def _gelu(x):
    return torch.nn.functional.gelu(x, approximate="tanh")
def _silu(x):
    return torch.nn.functional.silu(x)
def _sigmoid(x):
    return torch.sigmoid(x)
def _layernorm(x, axis=-1, eps=1e-5):
    m = x.mean(dim=axis, keepdim=True)
    v = x.var(dim=axis, keepdim=True, unbiased=False)
    return (x - m) / torch.sqrt(v + eps)
def _scatter_add(tgt, idx, src, axis=0):
    return tgt.clone().index_add_(axis, idx, src)
def _segment_sum(src, seg, n):
    out = torch.zeros((n,) + src.shape[1:], dtype=src.dtype)
    return out.index_add_(0, seg, src)
def _pad(x, pw):
    flat = tuple(v for pair in reversed(pw) for v in pair)
    return torch.nn.functional.pad(x, flat)

kernel_name = 'parallel_sum__gqa_block_cached__dense_activation_silu__decode'

def setup_inputs():

    _w = (lambda a: (lambda t: t.to(torch.bfloat16) if t.is_floating_point() else t)(torch.as_tensor(a)))
    def _A_setup():
        B, Sq, D = 2, 1, 1024
        Hq, Hkv, Sk, Dh = 32, 4, 32768, 128
        assert Hq % Hkv == 0, (Hq, Hkv)
        return {
            "x": _w((lambda *s: torch.randn(*s, dtype=torch.bfloat16) * 0.05)(B, Sq, D)),
            "k_cache": _w((lambda *s: torch.randn(*s, dtype=torch.bfloat16) * 3.2)(B, Hkv, Sk, Dh)),
            "v_cache": _w((lambda *s: torch.randn(*s, dtype=torch.bfloat16) * 0.8)(B, Hkv, Sk, Dh)),
            "wq": _w((lambda *s: torch.randn(*s, dtype=torch.bfloat16) * 3.2)(D, Hq * Dh) * (D ** -0.5)),
            "wo": _w((lambda *s: torch.randn(*s, dtype=torch.bfloat16) * 0.8)(Hq * Dh, D) * ((Hq * Dh) ** -0.5)),
        }
    _A_in = _A_setup()

    def _B_setup():
        B, S, D, F = 2, 1, 1024, 3584
        return {
            "x": _w((lambda *s: torch.randn(*s, dtype=torch.bfloat16) * 0.05)(B, S, D)),
            "w1": _w((lambda *s: torch.randn(*s, dtype=torch.bfloat16) * 3.2)(D, F) * (D ** -0.5)),
            "w2": _w((lambda *s: torch.randn(*s, dtype=torch.bfloat16) * 0.4)(F, D) * (F ** -0.5)),
        }
    _B_in = _B_setup()

    return {
        "A_x": _A_in["x"],
        "A_k_cache": _A_in["k_cache"],
        "A_v_cache": _A_in["v_cache"],
        "A_wq": _A_in["wq"],
        "A_wo": _A_in["wo"],
        "B_x": _B_in["x"],
        "B_w1": _B_in["w1"],
        "B_w2": _B_in["w2"],
    }

def reference(A_x, A_k_cache, A_v_cache, A_wq, A_wo, B_x, B_w1, B_w2):
    def _A_ref(x, k_cache, v_cache, wq, wo):
        B, Sq, D = x.shape
        Hq, Dh = 32, 128
        Hkv = k_cache.shape[1]
        rep = Hq // Hkv
        q = (lambda x, *d: x.permute(*d))((x @ wq).reshape(B, Sq, Hq, Dh), 0, 2, 1, 3)
        k = (lambda x, n, axis: torch.repeat_interleave(x, n, dim=axis))(k_cache, rep, axis=1)
        v = (lambda x, n, axis: torch.repeat_interleave(x, n, dim=axis))(v_cache, rep, axis=1)
        scores = torch.einsum("bhqd,bhkd->bhqk", q, k) * (Dh ** -0.5)
        p = _softmax(scores, axis=-1)
        o = torch.einsum("bhqk,bhkd->bhqd", p, v)
        return (lambda x, *d: x.permute(*d))(o, 0, 2, 1, 3).reshape(B, Sq, Hq * Dh) @ wo

    def _B_ref(x, w1, w2):
        h = torch.einsum("bsd,df->bsf", x, w1)
        h = _silu(h)
        out = torch.einsum("bsf,fd->bsd", h, w2)
        return out

    x = A_x
    xn = _layernorm(x, axis=-1)
    return x + _A_ref(xn, A_k_cache, A_v_cache, A_wq, A_wo) + _B_ref(xn, B_w1, B_w2)
```

```cpp
#include <hip/hip_runtime.h>
#include <hip/hip_bf16.h>
#include <stdint.h>

namespace {

typedef unsigned v4u __attribute__((ext_vector_type(4)));
typedef unsigned v2u __attribute__((ext_vector_type(2)));
typedef float f32x4 __attribute__((ext_vector_type(4)));
typedef short s16x4 __attribute__((ext_vector_type(4)));
typedef __bf16 bf16x8 __attribute__((ext_vector_type(8)));
typedef __bf16 bf16x2 __attribute__((ext_vector_type(2)));

constexpr int DM = 1024;
constexpr int NQ = 4096;
constexpr int SK = 32768;
constexpr int FF = 3584;
constexpr int NWG = 256, NTHR = 512;

constexpr unsigned WS_EPOCH = 0;
constexpr unsigned WS_QG = 4096;
constexpr unsigned WS_SG = WS_QG + 32768;
constexpr unsigned WS_PML = WS_SG + 32768;
constexpr unsigned WS_PO = WS_PML + 32768;
constexpr unsigned WS_P2 = WS_PO + 2u * 1024 * 1024;
constexpr unsigned WS_END = WS_P2 + 1024u * 1024;

constexpr int L_RQ = 0;
constexpr int L_RH = 1024;
constexpr int L_Q = 2048;
constexpr int L_ML = 4096;
constexpr int L_OC = 4608;
constexpr int L_S = 6656;
constexpr int L_FIN = 6912;
constexpr int L_RED = 7168;
constexpr int L_OR = 15360;
constexpr int OR_W = 4 * 272;
constexpr int L_BYTES = L_OR + 8 * OR_W * 4;

constexpr int AUX_SC1 = 16, AUX_NT = 2;
constexpr int SPIN_CAP = 1 << 16;
constexpr float LOG2E = 1.4426950408889634f;
constexpr float QK_SCALE = 0.08838834764831845f;

__device__ __forceinline__ float bf_lo(unsigned u) { return __uint_as_float(u << 16); }
__device__ __forceinline__ float bf_hi(unsigned u) { return __uint_as_float(u & 0xffff0000u); }
__device__ __forceinline__ unsigned cvt_pk(float lo, float hi) {
  bf16x2 p = {(__bf16)lo, (__bf16)hi};
  return __builtin_bit_cast(unsigned, p);
}
__device__ __forceinline__ float rbf(float x) { return bf_lo(cvt_pk(x, x)); }

template <int CTRL> __device__ __forceinline__ float dpp(float x) {
  return __builtin_bit_cast(float, __builtin_amdgcn_mov_dpp(__builtin_bit_cast(int, x), CTRL, 0xf, 0xf, true));
}
constexpr int XOR1 = 0xB1, XOR2 = 0x4E, HMIRROR = 0x141, MIRROR = 0x140, ROR8 = 0x128, ROR4 = 0x124;
__device__ __forceinline__ float x16_32_sum(float x) {
  auto s = __builtin_amdgcn_permlane16_swap(__float_as_uint(x), __float_as_uint(x), false, false);
  x = __uint_as_float(s[0]) + __uint_as_float(s[1]);
  auto t = __builtin_amdgcn_permlane32_swap(__float_as_uint(x), __float_as_uint(x), false, false);
  return __uint_as_float(t[0]) + __uint_as_float(t[1]);
}
__device__ __forceinline__ float x16_32_max(float x) {
  auto s = __builtin_amdgcn_permlane16_swap(__float_as_uint(x), __float_as_uint(x), false, false);
  x = fmaxf(__uint_as_float(s[0]), __uint_as_float(s[1]));
  auto t = __builtin_amdgcn_permlane32_swap(__float_as_uint(x), __float_as_uint(x), false, false);
  return fmaxf(__uint_as_float(t[0]), __uint_as_float(t[1]));
}
__device__ __forceinline__ float row16_sum(float x) {
  x += dpp<XOR1>(x); x += dpp<XOR2>(x); x += dpp<HMIRROR>(x); x += dpp<MIRROR>(x);
  return x;
}
__device__ __forceinline__ float row16_max(float x) {
  x = fmaxf(x, dpp<XOR1>(x)); x = fmaxf(x, dpp<XOR2>(x)); x = fmaxf(x, dpp<HMIRROR>(x)); x = fmaxf(x, dpp<MIRROR>(x));
  return x;
}
__device__ __forceinline__ float wave_sum(float x) { return x16_32_sum(row16_sum(x)); }
__device__ __forceinline__ float wave_max(float x) { return x16_32_max(row16_max(x)); }

__device__ __forceinline__ v4u ldnt(const void* p) { return __builtin_nontemporal_load((const v4u*)p); }
__device__ __forceinline__ f32x4 mfma32(v4u a, v4u b, f32x4 c) {
  return __builtin_amdgcn_mfma_f32_16x16x32_bf16(__builtin_bit_cast(bf16x8, a), __builtin_bit_cast(bf16x8, b), c, 0, 0, 0);
}
__device__ __forceinline__ f32x4 mfma16(v2u a, v2u b, f32x4 c) {
  return __builtin_amdgcn_mfma_f32_16x16x16bf16_1k(__builtin_bit_cast(s16x4, a), __builtin_bit_cast(s16x4, b), c, 0, 0, 0);
}
#define SB() __builtin_amdgcn_sched_barrier(0)
#define BAR() asm volatile("s_waitcnt lgkmcnt(0)\n\ts_barrier" ::: "memory")

__device__ __forceinline__ void slice_partial(const v4u (&wr)[4], const float (&xn)[2][4], float* red, int w, int lane) {
  float acc[16];
#pragma unroll
  for (int j = 0; j < 16; ++j) acc[j] = 0.f;
#pragma unroll
  for (int i = 0; i < 4; ++i) {
#pragma unroll
    for (int k = 0; k < 4; ++k) {
      const float lo = bf_lo(wr[i][k]), hi = bf_hi(wr[i][k]);
      acc[2 * k] = fmaf(xn[0][i], lo, acc[2 * k]);
      acc[2 * k + 1] = fmaf(xn[0][i], hi, acc[2 * k + 1]);
      acc[8 + 2 * k] = fmaf(xn[1][i], lo, acc[8 + 2 * k]);
      acc[8 + 2 * k + 1] = fmaf(xn[1][i], hi, acc[8 + 2 * k + 1]);
    }
  }
  float a8[8];
#pragma unroll
  for (int j = 0; j < 8; ++j) {
    auto s = __builtin_amdgcn_permlane32_swap(__float_as_uint(acc[j]), __float_as_uint(acc[8 + j]), false, false);
    a8[j] = __uint_as_float(s[0]) + __uint_as_float(s[1]);
  }
  float a4[4];
#pragma unroll
  for (int j = 0; j < 4; ++j) {
    auto s = __builtin_amdgcn_permlane16_swap(__float_as_uint(a8[j]), __float_as_uint(a8[4 + j]), false, false);
    a4[j] = __uint_as_float(s[0]) + __uint_as_float(s[1]);
  }
#pragma unroll
  for (int j = 0; j < 4; ++j) {
    a4[j] += dpp<ROR8>(a4[j]);
    a4[j] += dpp<ROR4>(a4[j]);
    a4[j] += dpp<XOR2>(a4[j]);
  }
  if ((lane & 14) == 0) {
    const int b = lane >> 5, r1 = (lane >> 4) & 1, half = lane & 1;
    *reinterpret_cast<f32x4*>(red + w * 32 + b * 16 + half * 8 + r1 * 4) = f32x4{a4[0], a4[1], a4[2], a4[3]};
  }
}

__global__ void __launch_bounds__(NTHR, 2)
fused_decode(const unsigned short* __restrict__ x, const unsigned short* __restrict__ kc, const unsigned short* __restrict__ vc,
             const unsigned short* __restrict__ wq, const unsigned short* __restrict__ wo, const unsigned short* __restrict__ w1,
             const unsigned short* __restrict__ w2, unsigned short* __restrict__ out, unsigned char* ws) {
  __shared__ __attribute__((aligned(16))) unsigned char lds[L_BYTES];
  const int tid = threadIdx.x, lane = tid & 63, w = __builtin_amdgcn_readfirstlane(tid >> 6);
  const int g = blockIdx.x, xc = g & 7, gi = g >> 3;
  const __amdgpu_buffer_rsrc_t rs = __builtin_amdgcn_make_buffer_rsrc(ws, 0, WS_END, 0x00020000);

  const unsigned epoch = __hip_atomic_load((const unsigned*)(ws + WS_EPOCH), __ATOMIC_RELAXED, __HIP_MEMORY_SCOPE_AGENT);
  v4u xs[4];
#pragma unroll
  for (int i = 0; i < 4; ++i) xs[i] = *reinterpret_cast<const v4u*>(x + (lane + 64 * i) * 8);
  unsigned short xr[2][4];
#pragma unroll
  for (int b = 0; b < 2; ++b)
#pragma unroll
    for (int i = 0; i < 4; ++i) xr[b][i] = x[b * DM + 128 * w + 32 * i + (lane >> 1)];
  SB();
  const int sq = xc * 32 + gi;
  const bool has_h = gi < 28;
  const int sh = xc * 28 + (has_h ? gi : gi - 28);
  v4u wqr[4], w1r[4];
  {
    const unsigned short* p = wq + (size_t)(128 * w + (lane >> 1)) * NQ + 16 * sq + 8 * (lane & 1);
#pragma unroll
    for (int i = 0; i < 4; ++i) wqr[i] = ldnt(p + (size_t)32 * i * NQ);
    SB();
    const unsigned short* p1 = w1 + (size_t)(128 * w + (lane >> 1)) * FF + 16 * sh + 8 * (lane & 1);
#pragma unroll
    for (int i = 0; i < 4; ++i) w1r[i] = ldnt(p1 + (size_t)32 * i * FF);
  }
  SB();
  const int ab = xc >> 2, kvh = xc & 3, split = gi;
  const size_t kvbase = ((size_t)(ab * 4 + kvh) * SK + (size_t)split * 1024 + 32 * w) * 128;
  const int r16 = lane & 15, gq = lane >> 4;
  const unsigned short* kp = kc + kvbase + (4 * (r16 & 3) + (r16 >> 2)) * 128 + gq * 8;
  const unsigned short* vp = vc + kvbase + lane * 8;
  v4u kr[4][8], vr[4][8];
#define ISSUE_K(c) do { _Pragma("unroll") for (int i_ = 0; i_ < 8; ++i_) kr[c][i_] = ldnt(kp + (c) * 256 * 128 + (i_ >> 2) * 16 * 128 + (i_ & 3) * 32); SB(); } while (0)
#define ISSUE_V(c) do { _Pragma("unroll") for (int i_ = 0; i_ < 8; ++i_) vr[c][i_] = ldnt(vp + (c) * 256 * 128 + i_ * 512); SB(); } while (0)
  ISSUE_K(0); ISSUE_V(0); ISSUE_K(1);

  float xn[2][4];
  {
    float s0 = 0.f, s1 = 0.f;
#pragma unroll
    for (int i = 0; i < 2; ++i)
#pragma unroll
      for (int k = 0; k < 4; ++k) { s0 += bf_lo(xs[i][k]) + bf_hi(xs[i][k]); s1 += bf_lo(xs[2 + i][k]) + bf_hi(xs[2 + i][k]); }
    const float mean0 = wave_sum(s0) * (1.f / DM), mean1 = wave_sum(s1) * (1.f / DM);
    float v0 = 0.f, v1 = 0.f;
#pragma unroll
    for (int i = 0; i < 2; ++i)
#pragma unroll
      for (int k = 0; k < 4; ++k) {
        float d;
        d = bf_lo(xs[i][k]) - mean0; v0 = fmaf(d, d, v0);
        d = bf_hi(xs[i][k]) - mean0; v0 = fmaf(d, d, v0);
        d = bf_lo(xs[2 + i][k]) - mean1; v1 = fmaf(d, d, v1);
        d = bf_hi(xs[2 + i][k]) - mean1; v1 = fmaf(d, d, v1);
      }
    const float var0 = rbf(wave_sum(v0) * (1.f / DM)), var1 = rbf(wave_sum(v1) * (1.f / DM));
    const float m0 = rbf(mean0), m1 = rbf(mean1);
    const float sd0 = rbf(sqrtf(rbf(var0 + 1e-5f))), sd1 = rbf(sqrtf(rbf(var1 + 1e-5f)));
#pragma unroll
    for (int i = 0; i < 4; ++i) {
      xn[0][i] = rbf(rbf(bf_lo(xr[0][i]) - m0) / sd0);
      xn[1][i] = rbf(rbf(bf_lo(xr[1][i]) - m1) / sd1);
    }
  }
  const unsigned tag = __builtin_amdgcn_readfirstlane(epoch) * 1664525u + 1013904223u;

  float* rq = reinterpret_cast<float*>(lds + L_RQ);
  float* rh = reinterpret_cast<float*>(lds + L_RH);
  slice_partial(wqr, xn, rq, w, lane);
  BAR();
  if (w == 0) {
    float s = 0.f;
    const int li = lane & 31;
#pragma unroll
    for (int ww = 0; ww < 8; ++ww) s += rq[ww * 32 + li];
    const float sn = dpp<XOR1>(s);
    const unsigned pk = cvt_pk(s, sn);
    if (lane < 32 && (lane & 1) == 0) {
      const int b = lane >> 4, c = lane & 15;
      v2u gr = {pk, tag};
      __builtin_amdgcn_raw_buffer_store_b64(gr, rs, WS_QG + (b * 2048 + sq * 8 + (c >> 1)) * 8, 0, AUX_SC1);
    }
  }
  slice_partial(w1r, xn, rh, w, lane);
  BAR();
  if (w == 1) {
    float s = 0.f;
    const int li = lane & 31;
#pragma unroll
    for (int ww = 0; ww < 8; ++ww) s += rh[ww * 32 + li];
    const float h = rbf(s);
    const float sv = h / (1.f + expf(-h));
    auto sw = __builtin_amdgcn_permlane16_swap(__float_as_uint(sv), __float_as_uint(sv), false, false);
    const unsigned pk = cvt_pk(__uint_as_float(sw[0]), __uint_as_float(sw[1]));
    if (lane < 16 && has_h) {
      v2u gr = {pk, tag};
      __builtin_amdgcn_raw_buffer_store_b64(gr, rs, WS_SG + (sh * 16 + lane) * 8, 0, AUX_SC1);
    }
  }
  SB();

  asm volatile("" ::"v"(vr[0][7]));
  SB();
  const unsigned qoff = WS_QG + (ab * 2048 + kvh * 512 + tid) * 8;
  v2u qg = __builtin_amdgcn_raw_buffer_load_b64(rs, qoff, 0, AUX_SC1);
  SB();
  ISSUE_V(1); ISSUE_K(2);
  if (!__all(qg[1] == tag)) {
    for (int spin = 0; spin < SPIN_CAP; ++spin) {
      asm volatile("" ::: "memory");
      qg = __builtin_amdgcn_raw_buffer_load_b64(rs, qoff, 0, AUX_SC1);
      if (__all(qg[1] == tag)) break;
      __builtin_amdgcn_s_sleep(2);
    }
  }
  reinterpret_cast<unsigned*>(lds + L_Q)[tid] = qg[0];
  BAR();
  v4u qf[4];
#pragma unroll
  for (int s = 0; s < 4; ++s) qf[s] = *reinterpret_cast<const v4u*>(lds + L_Q + (r16 & 7) * 256 + s * 64 + gq * 16);

  float m_run = -INFINITY, l_run = 0.f;
  f32x4 oacc[4];
#pragma unroll
  for (int a = 0; a < 4; ++a) oacc[a] = f32x4{0.f, 0.f, 0.f, 0.f};
  unsigned pk[8];
  const unsigned pmask = (lane & 8) ? 0xffff0000u : 0x0000ffffu;

#define QK(c) do { \
    f32x4 sc_[2]; \
    _Pragma("unroll") for (int t_ = 0; t_ < 2; ++t_) { \
      sc_[t_] = f32x4{0.f, 0.f, 0.f, 0.f}; \
      _Pragma("unroll") for (int s_ = 0; s_ < 4; ++s_) sc_[t_] = mfma32(kr[c][4 * t_ + s_], qf[s_], sc_[t_]); \
    } \
    float sv_[8]; float mx_ = -INFINITY; \
    _Pragma("unroll") for (int i_ = 0; i_ < 8; ++i_) { sv_[i_] = rbf(rbf(sc_[i_ >> 2][i_ & 3]) * QK_SCALE); mx_ = fmaxf(mx_, sv_[i_]); } \
    mx_ = x16_32_max(mx_); \
    const float mn_ = fmaxf(m_run, mx_); \
    const float al_ = __builtin_amdgcn_exp2f((m_run - mn_) * LOG2E); \
    m_run = mn_; \
    float ps_ = 0.f; \
    _Pragma("unroll") for (int i_ = 0; i_ < 8; ++i_) { \
      const float p_ = __builtin_amdgcn_exp2f((sv_[i_] - mn_) * LOG2E); ps_ += p_; pk[i_] = cvt_pk(p_, p_) & pmask; } \
    l_run = fmaf(l_run, al_, ps_); \
    _Pragma("unroll") for (int a_ = 0; a_ < 4; ++a_) oacc[a_] *= al_; \
  } while (0)
#define PV(c) do { \
    _Pragma("unroll") for (int i_ = 0; i_ < 8; ++i_) { \
      const v2u lo_ = {vr[c][i_][0], vr[c][i_][1]}, hi_ = {vr[c][i_][2], vr[c][i_][3]}; \
      const v2u b0_ = {pk[i_], 0u}, b1_ = {0u, pk[i_]}; \
      oacc[0] = mfma16(lo_, b0_, oacc[0]); oacc[1] = mfma16(lo_, b1_, oacc[1]); \
      oacc[2] = mfma16(hi_, b0_, oacc[2]); oacc[3] = mfma16(hi_, b1_, oacc[3]); \
    } \
  } while (0)

  const int rb = g >> 2, cb = g & 3;
  const int ch = w & 1, rg = w >> 1;
  v4u wor[4], w2r[4];

  QK(0); SB(); ISSUE_V(2);
  PV(0); SB(); ISSUE_K(3);
  QK(1); SB(); ISSUE_V(3);
  PV(1); SB();
  {
    const unsigned short* p = wo + (size_t)(64 * rb + 16 * rg + gq) * DM + 256 * cb + 128 * ch + 8 * r16;
#pragma unroll
    for (int i = 0; i < 4; ++i) wor[i] = ldnt(p + (size_t)4 * i * DM);
    SB();
  }
  QK(2); SB();
  {
#pragma unroll
    for (int i = 0; i < 4; ++i) {
      const int grp = rg + 4 * i;
      const int gc = grp < 14 ? grp : 13;
      w2r[i] = ldnt(w2 + (size_t)(56 * rb + 4 * gc + gq) * DM + 256 * cb + 128 * ch + 8 * r16);
    }
    SB();
  }
  PV(2); SB();
  QK(3); SB();
  PV(3); SB();

  {
    l_run = x16_32_sum(l_run);
    float* orw = reinterpret_cast<float*>(lds + L_OR) + w * OR_W + gq * 272 + r16 * 16;
#pragma unroll
    for (int a = 0; a < 4; ++a) *reinterpret_cast<f32x4*>(orw + 4 * a) = oacc[a];
    if (lane < 8) {
      float* ml = reinterpret_cast<float*>(lds + L_ML) + (w * 8 + lane) * 2;
      ml[0] = m_run; ml[1] = l_run;
    }
  }
  BAR();
  {
    const float* ml = reinterpret_cast<const float*>(lds + L_ML);
    float mw[8], M = -INFINITY;
#pragma unroll
    for (int ww = 0; ww < 8; ++ww) { mw[ww] = ml[(ww * 8 + w) * 2]; M = fmaxf(M, mw[ww]); }
    float L = 0.f, o0 = 0.f, o1 = 0.f;
    const float* orr = reinterpret_cast<const float*>(lds + L_OR) + (lane >> 4) * 272 + w * 16 + (lane & 3) * 4 + ((lane >> 2) & 3);
#pragma unroll
    for (int ww = 0; ww < 8; ++ww) {
      const float e = __builtin_amdgcn_exp2f((mw[ww] - M) * LOG2E);
      L = fmaf(e, ml[(ww * 8 + w) * 2 + 1], L);
      o0 = fmaf(e, orr[ww * OR_W], o0);
      o1 = fmaf(e, orr[ww * OR_W + 128], o1);
    }
    const int h = kvh * 8 + w;
    const unsigned slot = (unsigned)((ab * 32 + h) * 32 + split);
    v4u gr = {__float_as_uint(o0), tag, __float_as_uint(o1), tag};
    __builtin_amdgcn_raw_buffer_store_b128(gr, rs, WS_PO + slot * 1024 + lane * 16, 0, AUX_SC1);
    if (lane == 0) {
      v4u g2 = {__float_as_uint(M), tag, __float_as_uint(L), tag};
      __builtin_amdgcn_raw_buffer_store_b128(g2, rs, WS_PML + slot * 16, 0, AUX_SC1);
    }
  }

  {
    const int h = rb >> 1, dh = rb & 1;
    const int b = w >> 2, q4 = w & 3, sg = lane >> 5, dp = lane & 31;
    const unsigned slot0 = (unsigned)((b * 32 + h) * 32);
    const unsigned po = WS_PO + (slot0 + 8 * q4 + 4 * sg) * 1024 + (32 * dh + dp) * 16;
    const unsigned pm = WS_PML + (slot0 + dp) * 16;
    const unsigned ps = WS_SG + (56 * rb + (lane < 56 ? lane : 55)) * 8;
    v4u og[4], mg;
    v2u sgv;
    for (int spin = 0; spin < SPIN_CAP; ++spin) {
      asm volatile("" ::: "memory");
#pragma unroll
      for (int i = 0; i < 4; ++i) og[i] = __builtin_amdgcn_raw_buffer_load_b128(rs, po + i * 1024, 0, AUX_SC1);
      mg = __builtin_amdgcn_raw_buffer_load_b128(rs, pm, 0, AUX_SC1);
      sgv = __builtin_amdgcn_raw_buffer_load_b64(rs, ps, 0, AUX_SC1);
      bool ok = mg[1] == tag && mg[3] == tag && sgv[1] == tag;
#pragma unroll
      for (int i = 0; i < 4; ++i) ok = ok && og[i][1] == tag && og[i][3] == tag;
      if (__all(ok)) break;
      __builtin_amdgcn_s_sleep(1);
    }
    const float ms = __uint_as_float(mg[0]), ls = __uint_as_float(mg[2]);
    const float M = row16_max(ms);
    const float Mx = x16_32_max(M);
    const float e = __builtin_amdgcn_exp2f((ms - Mx) * LOG2E);
    float L = row16_sum(e * ls);
    {
      auto s = __builtin_amdgcn_permlane16_swap(__float_as_uint(L), __float_as_uint(L), false, false);
      L = __uint_as_float(s[0]) + __uint_as_float(s[1]);
    }
    const float wgt = e / L;
    float o0 = 0.f, o1 = 0.f;
#pragma unroll
    for (int i = 0; i < 4; ++i) {
      const float wi = __shfl(wgt, 8 * q4 + 4 * sg + i, 64);
      o0 = fmaf(wi, __uint_as_float(og[i][0]), o0);
      o1 = fmaf(wi, __uint_as_float(og[i][2]), o1);
    }
    {
      auto s0 = __builtin_amdgcn_permlane32_swap(__float_as_uint(o0), __float_as_uint(o0), false, false);
      auto s1 = __builtin_amdgcn_permlane32_swap(__float_as_uint(o1), __float_as_uint(o1), false, false);
      o0 = __uint_as_float(s0[0]) + __uint_as_float(s0[1]);
      o1 = __uint_as_float(s1[0]) + __uint_as_float(s1[1]);
    }
    float* oc = reinterpret_cast<float*>(lds + L_OC);
    if (lane < 32) { oc[(b * 64 + 2 * dp) * 4 + q4] = o0; oc[(b * 64 + 2 * dp + 1) * 4 + q4] = o1; }
    if (w == 0) reinterpret_cast<unsigned*>(lds + L_S)[lane] = sgv[0];
  }
  BAR();
  {
    const int b = r16 >> 3, j0 = r16 & 7;
    const unsigned sh16 = (j0 & 1) * 16;
    unsigned msk[4];
#pragma unroll
    for (int r = 0; r < 4; ++r) msk[r] = (r == (j0 >> 1)) ? 0xffffffffu : 0u;
    f32x4 acc = {0.f, 0.f, 0.f, 0.f};
    const float* oc = reinterpret_cast<const float*>(lds + L_OC);
#pragma unroll
    for (int i = 0; i < 4; ++i) {
      const f32x4 o4 = *reinterpret_cast<const f32x4*>(oc + (b * 64 + 16 * rg + 4 * i + gq) * 4);
      const float o = (o4[0] + o4[1]) + (o4[2] + o4[3]);
      const unsigned v = (cvt_pk(o, o) & 0xffffu) << sh16;
      const v4u bf = {v & msk[0], v & msk[1], v & msk[2], v & msk[3]};
      acc = mfma32(wor[i], bf, acc);
    }
#pragma unroll
    for (int i = 0; i < 4; ++i) {
      const int grp = rg + 4 * i;
      const unsigned sp = reinterpret_cast<const unsigned*>(lds + L_S)[(grp < 14 ? 4 * grp : 0) + gq];
      unsigned v = ((b ? (sp >> 16) : sp) & 0xffffu) << sh16;
      if (grp >= 14) v = 0u;
      const v4u bf = {v & msk[0], v & msk[1], v & msk[2], v & msk[3]};
      acc = mfma32(w2r[i], bf, acc);
    }
    *reinterpret_cast<f32x4*>(lds + L_RED + (w * 64 + lane) * 16) = acc;
  }
  BAR();
  if (tid < 256) {
    const int b = tid >> 7, n = (tid & 127) * 2;
    const int chh = n >> 7, c = (n & 127) >> 3, j0 = n & 7;
    const float* red = reinterpret_cast<const float*>(lds + L_RED);
    const int sl = (c >> 2) * 16 + b * 8 + j0;
    float v0 = 0.f, v1 = 0.f;
#pragma unroll
    for (int r = 0; r < 4; ++r) {
      v0 += red[((r * 2 + chh) * 64 + sl) * 4 + (c & 3)];
      v1 += red[((r * 2 + chh) * 64 + sl + 1) * 4 + (c & 3)];
    }
    v4u gr = {__float_as_uint(v0), tag, __float_as_uint(v1), tag};
    __builtin_amdgcn_raw_buffer_store_b128(gr, rs, WS_P2 + ((rb * 2 + b) * 1024 + 256 * cb + n) * 8, 0, AUX_SC1);
  }

  {
    const int b = g & 1, c0 = (g >> 1) * 8;
    const unsigned off = WS_P2 + (((tid >> 3) * 2 + b) * 1024 + c0 + (tid & 7)) * 8;
    const float xres = bf_lo(x[b * DM + c0 + (tid & 7)]);
    v2u pg;
    for (int spin = 0; spin < SPIN_CAP; ++spin) {
      asm volatile("" ::: "memory");
      pg = __builtin_amdgcn_raw_buffer_load_b64(rs, off, 0, AUX_SC1);
      if (__all(pg[1] == tag)) break;
      __builtin_amdgcn_s_sleep(1);
    }
    float v = __uint_as_float(pg[0]);
    v += dpp<ROR8>(v);
    v = x16_32_sum(v);
    float* fin = reinterpret_cast<float*>(lds + L_FIN);
    if (lane < 8) fin[w * 8 + lane] = v;
    BAR();
    if (tid < 8) {
      float s = 0.f;
#pragma unroll
      for (int ww = 0; ww < 8; ++ww) s += fin[ww * 8 + tid];
      out[b * DM + c0 + tid] = (unsigned short)(cvt_pk(xres + s, 0.f) & 0xffffu);
    }
    if (g == 0 && tid == 0) __hip_atomic_store((unsigned*)(ws + WS_EPOCH), tag, __ATOMIC_RELAXED, __HIP_MEMORY_SCOPE_AGENT);
  }
}

}

extern "C" void kernel_launch(void* const* d_in, const int* in_sizes, int n_in, void* d_out, int out_size, void* d_ws, size_t ws_size,
                              hipStream_t stream) {
  (void)in_sizes; (void)n_in; (void)out_size; (void)ws_size;
  hipLaunchKernelGGL(fused_decode, dim3(NWG), dim3(NTHR), 0, stream, (const unsigned short*)d_in[0], (const unsigned short*)d_in[1],
                     (const unsigned short*)d_in[2], (const unsigned short*)d_in[3], (const unsigned short*)d_in[4],
                     (const unsigned short*)d_in[6], (const unsigned short*)d_in[7], (unsigned short*)d_out, (unsigned char*)d_ws);
}
```

```cpp
#include <hip/hip_runtime.h>
#include <hip/hip_bf16.h>
#include <stdint.h>

namespace {

typedef unsigned v4u __attribute__((ext_vector_type(4)));
typedef unsigned v2u __attribute__((ext_vector_type(2)));
typedef float f32x4 __attribute__((ext_vector_type(4)));
typedef short s16x4 __attribute__((ext_vector_type(4)));
typedef __bf16 bf16x8 __attribute__((ext_vector_type(8)));
typedef __bf16 bf16x2 __attribute__((ext_vector_type(2)));

constexpr int DM = 1024;
constexpr int NQ = 4096;
constexpr int SK = 32768;
constexpr int FF = 3584;
constexpr int NWG = 256, NTHR = 512;

constexpr unsigned WS_EPOCH = 0;
constexpr unsigned WS_QG = 4096;
constexpr unsigned WS_SG = WS_QG + 131072;
constexpr unsigned WS_PML = WS_SG + 131072;
constexpr unsigned WS_PO = WS_PML + 32768;
constexpr unsigned WS_P2 = WS_PO + 2u * 1024 * 1024;
constexpr unsigned WS_END = WS_P2 + 1024u * 1024;

constexpr int L_RQ = 0;
constexpr int L_Q = 8192;
constexpr int L_ML = 10240;
constexpr int L_OC = 10752;
constexpr int L_S = 12800;
constexpr int L_FIN = 14848;
constexpr int L_RED = 15104;
constexpr int L_OR = 23296;
constexpr int OR_W = 4 * 272;
constexpr int L_BYTES = L_OR + 8 * OR_W * 4;

constexpr int AUX_SC1 = 16, AUX_NT = 2;
constexpr int SPIN_CAP = 1 << 16;
constexpr float LOG2E = 1.4426950408889634f;
constexpr float QK_SCALE = 0.08838834764831845f;

__device__ __forceinline__ float bf_lo(unsigned u) { return __uint_as_float(u << 16); }
__device__ __forceinline__ float bf_hi(unsigned u) { return __uint_as_float(u & 0xffff0000u); }
__device__ __forceinline__ unsigned cvt_pk(float lo, float hi) {
  bf16x2 p = {(__bf16)lo, (__bf16)hi};
  return __builtin_bit_cast(unsigned, p);
}
__device__ __forceinline__ float rbf(float x) { return bf_lo(cvt_pk(x, x)); }

template <int CTRL> __device__ __forceinline__ float dpp(float x) {
  return __builtin_bit_cast(float, __builtin_amdgcn_mov_dpp(__builtin_bit_cast(int, x), CTRL, 0xf, 0xf, true));
}
constexpr int XOR1 = 0xB1, XOR2 = 0x4E, HMIRROR = 0x141, MIRROR = 0x140, ROR8 = 0x128, ROR4 = 0x124;
__device__ __forceinline__ float x16_32_sum(float x) {
  auto s = __builtin_amdgcn_permlane16_swap(__float_as_uint(x), __float_as_uint(x), false, false);
  x = __uint_as_float(s[0]) + __uint_as_float(s[1]);
  auto t = __builtin_amdgcn_permlane32_swap(__float_as_uint(x), __float_as_uint(x), false, false);
  return __uint_as_float(t[0]) + __uint_as_float(t[1]);
}
__device__ __forceinline__ float x16_32_max(float x) {
  auto s = __builtin_amdgcn_permlane16_swap(__float_as_uint(x), __float_as_uint(x), false, false);
  x = fmaxf(__uint_as_float(s[0]), __uint_as_float(s[1]));
  auto t = __builtin_amdgcn_permlane32_swap(__float_as_uint(x), __float_as_uint(x), false, false);
  return fmaxf(__uint_as_float(t[0]), __uint_as_float(t[1]));
}
__device__ __forceinline__ float row16_sum(float x) {
  x += dpp<XOR1>(x); x += dpp<XOR2>(x); x += dpp<HMIRROR>(x); x += dpp<MIRROR>(x);
  return x;
}
__device__ __forceinline__ float row16_max(float x) {
  x = fmaxf(x, dpp<XOR1>(x)); x = fmaxf(x, dpp<XOR2>(x)); x = fmaxf(x, dpp<HMIRROR>(x)); x = fmaxf(x, dpp<MIRROR>(x));
  return x;
}
__device__ __forceinline__ float wave_sum(float x) { return x16_32_sum(row16_sum(x)); }
__device__ __forceinline__ float wave_max(float x) { return x16_32_max(row16_max(x)); }

__device__ __forceinline__ v4u ldnt(const void* p) { return __builtin_nontemporal_load((const v4u*)p); }
#define WPOL 1
__device__ __forceinline__ v4u ldw(const void* p) { if (WPOL) return __builtin_nontemporal_load((const v4u*)p); else return *(const v4u*)p; }
__device__ __forceinline__ f32x4 mfma32(v4u a, v4u b, f32x4 c) {
  return __builtin_amdgcn_mfma_f32_16x16x32_bf16(__builtin_bit_cast(bf16x8, a), __builtin_bit_cast(bf16x8, b), c, 0, 0, 0);
}
__device__ __forceinline__ f32x4 mfma16(v2u a, v2u b, f32x4 c) {
  return __builtin_amdgcn_mfma_f32_16x16x16bf16_1k(__builtin_bit_cast(s16x4, a), __builtin_bit_cast(s16x4, b), c, 0, 0, 0);
}
#define SB() __builtin_amdgcn_sched_barrier(0)
#define WSTAMP(i) do { } while (0)
#define STAMP(i) do { } while (0)
#define BAR() asm volatile("s_waitcnt lgkmcnt(0)\n\ts_barrier" ::: "memory")

__device__ __forceinline__ void slice_reduce(const float (&acc)[16], float* red, int w, int lane) {
  float a8[8];
#pragma unroll
  for (int j = 0; j < 8; ++j) {
    auto s = __builtin_amdgcn_permlane32_swap(__float_as_uint(acc[j]), __float_as_uint(acc[8 + j]), false, false);
    a8[j] = __uint_as_float(s[0]) + __uint_as_float(s[1]);
  }
  float a4[4];
#pragma unroll
  for (int j = 0; j < 4; ++j) {
    auto s = __builtin_amdgcn_permlane16_swap(__float_as_uint(a8[j]), __float_as_uint(a8[4 + j]), false, false);
    a4[j] = __uint_as_float(s[0]) + __uint_as_float(s[1]);
  }
#pragma unroll
  for (int j = 0; j < 4; ++j) a4[j] += dpp<ROR8>(a4[j]);
  if ((lane & 8) == 0) {
    const int b = lane >> 5, r1 = (lane >> 4) & 1, piece = lane & 7;
    *reinterpret_cast<f32x4*>(red + w * 128 + b * 64 + piece * 8 + r1 * 4) = f32x4{a4[0], a4[1], a4[2], a4[3]};
  }
}

__global__ void __launch_bounds__(NTHR, 2)
fused_decode(const unsigned short* __restrict__ x, const unsigned short* __restrict__ kc, const unsigned short* __restrict__ vc,
             const unsigned short* __restrict__ wq, const unsigned short* __restrict__ wo, const unsigned short* __restrict__ w1,
             const unsigned short* __restrict__ w2, unsigned short* __restrict__ out, unsigned char* ws) {
  __shared__ __attribute__((aligned(16))) unsigned char lds[L_BYTES];
  const int tid = threadIdx.x, lane = tid & 63, w = __builtin_amdgcn_readfirstlane(tid >> 6);
  const int g = blockIdx.x, xc = g & 7, gi = g >> 3;
  const __amdgpu_buffer_rsrc_t rs = __builtin_amdgcn_make_buffer_rsrc(ws, 0, WS_END, 0x00020000);

  STAMP(0);
  const int ab = xc >> 2, kvh = xc & 3, split = gi;
  const size_t kvbase = ((size_t)(ab * 4 + kvh) * SK + (size_t)split * 1024 + 32 * w) * 128;
  const int r16 = lane & 15, gq = lane >> 4;
  const unsigned short* kp = kc + kvbase + (4 * (r16 & 3) + (r16 >> 2)) * 128 + gq * 8;
  const unsigned short* vp = vc + kvbase + lane * 8;
  v4u kr[4][8], vr[4][8];
#define LD_K(c, i_) kr[c][i_] = ldnt(kp + (c) * 256 * 128 + ((i_) >> 2) * 16 * 128 + ((i_) & 3) * 32)
#define ISSUE_K(c) do { _Pragma("unroll") for (int i_ = 0; i_ < 8; ++i_) LD_K(c, i_); SB(); } while (0)
#define ISSUE_V(c) do { _Pragma("unroll") for (int i_ = 0; i_ < 8; ++i_) vr[c][i_] = ldnt(vp + (c) * 256 * 128 + i_ * 512); SB(); } while (0)

  const unsigned epoch = __hip_atomic_load((const unsigned*)(ws + WS_EPOCH), __ATOMIC_RELAXED, __HIP_MEMORY_SCOPE_AGENT);
  unsigned tag;
  {
    const bool is_q = g < 128, has_a = g < 240;
    const int u = is_q ? g : g - 128;
    const int ar = u & 1, ac = u >> 1;
    const int ld = is_q ? NQ : FF;
    const __amdgpu_buffer_rsrc_t rw = __builtin_amdgcn_make_buffer_rsrc((void*)(is_q ? wq : w1), 0, has_a ? DM * ld * 2 : 0, 0x00020000);
    v4u xs[4];
#pragma unroll
    for (int i = 0; i < 4; ++i) xs[i] = *reinterpret_cast<const v4u*>(x + (lane + 64 * i) * 8);
    const int xrow = 512 * ar + 64 * w + lane;
    const unsigned short xr0 = x[xrow], xr1 = x[DM + xrow];
    SB();
    v4u wr[8];
    {
      const int off = ((512 * ar + 64 * w + (lane >> 3)) * ld + 64 * ac + 8 * (lane & 7)) * 2;
#pragma unroll
      for (int i = 0; i < 8; ++i) wr[i] = __builtin_amdgcn_raw_buffer_load_b128(rw, off, 16 * i * ld, AUX_NT);
    }
    SB();
    float s0 = 0.f, s1 = 0.f;
#pragma unroll
    for (int i = 0; i < 2; ++i)
#pragma unroll
      for (int k = 0; k < 4; ++k) { s0 += bf_lo(xs[i][k]) + bf_hi(xs[i][k]); s1 += bf_lo(xs[2 + i][k]) + bf_hi(xs[2 + i][k]); }
    const float mean0 = wave_sum(s0) * (1.f / DM), mean1 = wave_sum(s1) * (1.f / DM);
    float v0 = 0.f, v1 = 0.f;
#pragma unroll
    for (int i = 0; i < 2; ++i)
#pragma unroll
      for (int k = 0; k < 4; ++k) {
        float d;
        d = bf_lo(xs[i][k]) - mean0; v0 = fmaf(d, d, v0);
        d = bf_hi(xs[i][k]) - mean0; v0 = fmaf(d, d, v0);
        d = bf_lo(xs[2 + i][k]) - mean1; v1 = fmaf(d, d, v1);
        d = bf_hi(xs[2 + i][k]) - mean1; v1 = fmaf(d, d, v1);
      }
    const float var0 = rbf(wave_sum(v0) * (1.f / DM)), var1 = rbf(wave_sum(v1) * (1.f / DM));
    const float m0 = rbf(mean0), m1 = rbf(mean1);
    const float sd0 = rbf(sqrtf(rbf(var0 + 1e-5f))), sd1 = rbf(sqrtf(rbf(var1 + 1e-5f)));
    const unsigned xnp = cvt_pk(rbf(bf_lo(xr0) - m0) / sd0, rbf(bf_lo(xr1) - m1) / sd1);
    unsigned xb[8];
#pragma unroll
    for (int i = 0; i < 8; ++i) xb[i] = __builtin_amdgcn_ds_bpermute((8 * i + (lane >> 3)) * 4, xnp);
    tag = __builtin_amdgcn_readfirstlane(epoch) * 1664525u + 1013904223u;
    SB();
    float acc[16];
#pragma unroll
    for (int j = 0; j < 16; ++j) acc[j] = 0.f;
#pragma unroll
    for (int i = 0; i < 8; ++i) {
      const float x0 = bf_lo(xb[i]), x1 = bf_hi(xb[i]);
#pragma unroll
      for (int k = 0; k < 4; ++k) {
        const float lo = bf_lo(wr[i][k]), hi = bf_hi(wr[i][k]);
        acc[2 * k] = fmaf(x0, lo, acc[2 * k]);
        acc[2 * k + 1] = fmaf(x0, hi, acc[2 * k + 1]);
        acc[8 + 2 * k] = fmaf(x1, lo, acc[8 + 2 * k]);
        acc[8 + 2 * k + 1] = fmaf(x1, hi, acc[8 + 2 * k + 1]);
      }
      SB();
      LD_K(0, i);
      SB();
    }
    float* rq = reinterpret_cast<float*>(lds + L_RQ);
    slice_reduce(acc, rq, w, lane);
    BAR();
    if (w == 0 && has_a) {
      float p0 = 0.f, p1 = 0.f;
#pragma unroll
      for (int ww = 0; ww < 8; ++ww) {
        const float2 v = *reinterpret_cast<const float2*>(rq + ww * 128 + 2 * lane);
        p0 += v.x; p1 += v.y;
      }
      const unsigned off = (is_q ? WS_QG : WS_SG) + (((lane >> 5) * 2 + ar) * ld + 64 * ac + 2 * (lane & 31)) * 8;
      v4u gr = {__float_as_uint(p0), tag, __float_as_uint(p1), tag};
      __builtin_amdgcn_raw_buffer_store_b128(gr, rs, off, 0, AUX_SC1);
    }
    SB();
  }
  STAMP(1);
  ISSUE_V(0);
  STAMP(2);

  const unsigned qoff = WS_QG + (ab * 2 * NQ + kvh * 1024 + 2 * tid) * 8;
  v4u qg[2];
#pragma unroll
  for (int r = 0; r < 2; ++r) qg[r] = __builtin_amdgcn_raw_buffer_load_b128(rs, qoff + r * NQ * 8, 0, AUX_SC1);
  SB();
  asm volatile("s_barrier" ::: "memory");
  SB();
  ISSUE_K(1);
  STAMP(3);
#define QG_OK() (qg[0][1] == tag && qg[0][3] == tag && qg[1][1] == tag && qg[1][3] == tag)
  if (!__all(QG_OK())) {
    for (int spin = 0; spin < SPIN_CAP; ++spin) {
      asm volatile("" ::: "memory");
#pragma unroll
      for (int r = 0; r < 2; ++r) qg[r] = __builtin_amdgcn_raw_buffer_load_b128(rs, qoff + r * NQ * 8, 0, AUX_SC1);
      if (__all(QG_OK())) break;
      __builtin_amdgcn_s_sleep(2);
    }
  }
  reinterpret_cast<unsigned*>(lds + L_Q)[tid] =
      cvt_pk(__uint_as_float(qg[0][0]) + __uint_as_float(qg[1][0]), __uint_as_float(qg[0][2]) + __uint_as_float(qg[1][2]));
  BAR();
  STAMP(4);
  v4u qf[4];
#pragma unroll
  for (int s = 0; s < 4; ++s) qf[s] = *reinterpret_cast<const v4u*>(lds + L_Q + (r16 & 7) * 256 + s * 64 + gq * 16);

  float m_run = -INFINITY, l_run = 0.f;
  f32x4 oacc[4];
#pragma unroll
  for (int a = 0; a < 4; ++a) oacc[a] = f32x4{0.f, 0.f, 0.f, 0.f};
  unsigned pk[8];
  const unsigned pmask = (lane & 8) ? 0xffff0000u : 0x0000ffffu;

#define QK(c) do { \
    f32x4 sc_[2]; \
    _Pragma("unroll") for (int t_ = 0; t_ < 2; ++t_) { \
      sc_[t_] = f32x4{0.f, 0.f, 0.f, 0.f}; \
      _Pragma("unroll") for (int s_ = 0; s_ < 4; ++s_) sc_[t_] = mfma32(kr[c][4 * t_ + s_], qf[s_], sc_[t_]); \
    } \
    float sv_[8]; float mx_ = -INFINITY; \
    _Pragma("unroll") for (int i_ = 0; i_ < 8; ++i_) { sv_[i_] = rbf(rbf(sc_[i_ >> 2][i_ & 3]) * QK_SCALE); mx_ = fmaxf(mx_, sv_[i_]); } \
    mx_ = x16_32_max(mx_); \
    const float mn_ = fmaxf(m_run, mx_); \
    const float al_ = __builtin_amdgcn_exp2f((m_run - mn_) * LOG2E); \
    m_run = mn_; \
    float ps_ = 0.f; \
    _Pragma("unroll") for (int i_ = 0; i_ < 8; ++i_) { \
      const float p_ = __builtin_amdgcn_exp2f((sv_[i_] - mn_) * LOG2E); ps_ += p_; pk[i_] = cvt_pk(p_, p_) & pmask; } \
    l_run = fmaf(l_run, al_, ps_); \
    _Pragma("unroll") for (int a_ = 0; a_ < 4; ++a_) oacc[a_] *= al_; \
  } while (0)
#define PV(c) do { \
    _Pragma("unroll") for (int i_ = 0; i_ < 8; ++i_) { \
      const v2u lo_ = {vr[c][i_][0], vr[c][i_][1]}, hi_ = {vr[c][i_][2], vr[c][i_][3]}; \
      const v2u b0_ = {pk[i_], 0u}, b1_ = {0u, pk[i_]}; \
      oacc[0] = mfma16(lo_, b0_, oacc[0]); oacc[1] = mfma16(lo_, b1_, oacc[1]); \
      oacc[2] = mfma16(hi_, b0_, oacc[2]); oacc[3] = mfma16(hi_, b1_, oacc[3]); \
    } \
  } while (0)

  const int rb = g >> 2, cb = g & 3;
  const int ch = w & 1, rg = w >> 1;
  v4u wor[4], w2r[4];

  WSTAMP(0);
  QK(0); SB(); WSTAMP(1); ISSUE_V(1); WSTAMP(2);
  PV(0); SB(); WSTAMP(3); ISSUE_K(2); WSTAMP(4);
  QK(1); SB(); WSTAMP(5); ISSUE_V(2); WSTAMP(6);
  PV(1); SB(); WSTAMP(7); ISSUE_K(3); WSTAMP(8);
  QK(2); SB(); WSTAMP(9); ISSUE_V(3); WSTAMP(10);
  PV(2); SB(); WSTAMP(11);
  {
    const unsigned short* p = wo + (size_t)(64 * rb + 16 * rg + gq) * DM + 256 * cb + 128 * ch + 8 * r16;
#pragma unroll
    for (int i = 0; i < 4; ++i) wor[i] = ldnt(p + (size_t)4 * i * DM);
    SB();
  }
  WSTAMP(12);
  QK(3); SB(); WSTAMP(13);
  {
#pragma unroll
    for (int i = 0; i < 4; ++i) {
      const int grp = rg + 4 * i;
      const int gc = grp < 14 ? grp : 13;
      w2r[i] = ldnt(w2 + (size_t)(56 * rb + 4 * gc + gq) * DM + 256 * cb + 128 * ch + 8 * r16);
    }
    SB();
  }
  WSTAMP(14);
  PV(3); SB();
  WSTAMP(15);

  STAMP(5);
  {
    l_run = x16_32_sum(l_run);
    float* orw = reinterpret_cast<float*>(lds + L_OR) + w * OR_W + gq * 272 + r16 * 16;
#pragma unroll
    for (int a = 0; a < 4; ++a) *reinterpret_cast<f32x4*>(orw + 4 * a) = oacc[a];
    if (lane < 8) {
      float* ml = reinterpret_cast<float*>(lds + L_ML) + (w * 8 + lane) * 2;
      ml[0] = m_run; ml[1] = l_run;
    }
  }
  BAR();
  {
    const float* ml = reinterpret_cast<const float*>(lds + L_ML);
    float mw[8], M = -INFINITY;
#pragma unroll
    for (int ww = 0; ww < 8; ++ww) { mw[ww] = ml[(ww * 8 + w) * 2]; M = fmaxf(M, mw[ww]); }
    float L = 0.f, o0 = 0.f, o1 = 0.f;
    const float* orr = reinterpret_cast<const float*>(lds + L_OR) + (lane >> 4) * 272 + w * 16 + (lane & 3) * 4 + ((lane >> 2) & 3);
#pragma unroll
    for (int ww = 0; ww < 8; ++ww) {
      const float e = __builtin_amdgcn_exp2f((mw[ww] - M) * LOG2E);
      L = fmaf(e, ml[(ww * 8 + w) * 2 + 1], L);
      o0 = fmaf(e, orr[ww * OR_W], o0);
      o1 = fmaf(e, orr[ww * OR_W + 128], o1);
    }
    const int h = kvh * 8 + w;
    const unsigned slot = (unsigned)((ab * 32 + h) * 32 + split);
    v4u gr = {__float_as_uint(o0), tag, __float_as_uint(o1), tag};
    __builtin_amdgcn_raw_buffer_store_b128(gr, rs, WS_PO + slot * 1024 + lane * 16, 0, AUX_SC1);
    if (lane == 0) {
      v4u g2 = {__float_as_uint(M), tag, __float_as_uint(L), tag};
      __builtin_amdgcn_raw_buffer_store_b128(g2, rs, WS_PML + slot * 16, 0, AUX_SC1);
    }
  }

  STAMP(6);
  {
    const int h = rb >> 1, dh = rb & 1;
    const int b = w >> 2, q4 = w & 3, sg = lane >> 5, dp = lane & 31;
    const unsigned slot0 = (unsigned)((b * 32 + h) * 32);
    const unsigned po = WS_PO + (slot0 + 8 * q4 + 4 * sg) * 1024 + (32 * dh + dp) * 16;
    const unsigned pm = WS_PML + (slot0 + dp) * 16;
    const unsigned ps = WS_SG + ((b * 2 + (q4 & 1)) * FF + 56 * rb + (lane < 56 ? lane : 55)) * 8;
    v4u og[4], mg;
    v2u sgv;
    for (int spin = 0; spin < SPIN_CAP; ++spin) {
      asm volatile("" ::: "memory");
#pragma unroll
      for (int i = 0; i < 4; ++i) og[i] = __builtin_amdgcn_raw_buffer_load_b128(rs, po + i * 1024, 0, AUX_SC1);
      mg = __builtin_amdgcn_raw_buffer_load_b128(rs, pm, 0, AUX_SC1);
      sgv = __builtin_amdgcn_raw_buffer_load_b64(rs, ps, 0, AUX_SC1);
      bool ok = mg[1] == tag && mg[3] == tag && sgv[1] == tag;
#pragma unroll
      for (int i = 0; i < 4; ++i) ok = ok && og[i][1] == tag && og[i][3] == tag;
      if (__all(ok)) break;
      __builtin_amdgcn_s_sleep(1);
    }
    STAMP(7);
    const float ms = __uint_as_float(mg[0]), ls = __uint_as_float(mg[2]);
    const float M = row16_max(ms);
    const float Mx = x16_32_max(M);
    const float e = __builtin_amdgcn_exp2f((ms - Mx) * LOG2E);
    float L = row16_sum(e * ls);
    {
      auto s = __builtin_amdgcn_permlane16_swap(__float_as_uint(L), __float_as_uint(L), false, false);
      L = __uint_as_float(s[0]) + __uint_as_float(s[1]);
    }
    const float wgt = e / L;
    float o0 = 0.f, o1 = 0.f;
#pragma unroll
    for (int i = 0; i < 4; ++i) {
      const float wi = __shfl(wgt, 8 * q4 + 4 * sg + i, 64);
      o0 = fmaf(wi, __uint_as_float(og[i][0]), o0);
      o1 = fmaf(wi, __uint_as_float(og[i][2]), o1);
    }
    {
      auto s0 = __builtin_amdgcn_permlane32_swap(__float_as_uint(o0), __float_as_uint(o0), false, false);
      auto s1 = __builtin_amdgcn_permlane32_swap(__float_as_uint(o1), __float_as_uint(o1), false, false);
      o0 = __uint_as_float(s0[0]) + __uint_as_float(s0[1]);
      o1 = __uint_as_float(s1[0]) + __uint_as_float(s1[1]);
    }
    float* oc = reinterpret_cast<float*>(lds + L_OC);
    if (lane < 32) { oc[(b * 64 + 2 * dp) * 4 + q4] = o0; oc[(b * 64 + 2 * dp + 1) * 4 + q4] = o1; }
    if (q4 < 2) reinterpret_cast<unsigned*>(lds + L_S)[(b * 64 + lane) * 2 + q4] = sgv[0];
  }
  BAR();
  {
    const int b = r16 >> 3, j0 = r16 & 7;
    const unsigned sh16 = (j0 & 1) * 16;
    unsigned msk[4];
#pragma unroll
    for (int r = 0; r < 4; ++r) msk[r] = (r == (j0 >> 1)) ? 0xffffffffu : 0u;
    f32x4 acc = {0.f, 0.f, 0.f, 0.f};
    const float* oc = reinterpret_cast<const float*>(lds + L_OC);
#pragma unroll
    for (int i = 0; i < 4; ++i) {
      const f32x4 o4 = *reinterpret_cast<const f32x4*>(oc + (b * 64 + 16 * rg + 4 * i + gq) * 4);
      const float o = (o4[0] + o4[1]) + (o4[2] + o4[3]);
      const unsigned v = (cvt_pk(o, o) & 0xffffu) << sh16;
      const v4u bf = {v & msk[0], v & msk[1], v & msk[2], v & msk[3]};
      acc = mfma32(wor[i], bf, acc);
    }
#pragma unroll
    for (int i = 0; i < 4; ++i) {
      const int grp = rg + 4 * i;
      const float2 h2 = *reinterpret_cast<const float2*>(lds + L_S + (b * 64 + (grp < 14 ? 4 * grp : 0) + gq) * 8);
      const float hv = rbf(h2.x + h2.y);
      const float sv = hv / (1.f + __expf(-hv));
      unsigned v = (cvt_pk(sv, sv) & 0xffffu) << sh16;
      if (grp >= 14) v = 0u;
      const v4u bf = {v & msk[0], v & msk[1], v & msk[2], v & msk[3]};
      acc = mfma32(w2r[i], bf, acc);
    }
    *reinterpret_cast<f32x4*>(lds + L_RED + (w * 64 + lane) * 16) = acc;
  }
  BAR();
  if (tid < 256) {
    const int b = tid >> 7, n = (tid & 127) * 2;
    const int chh = n >> 7, c = (n & 127) >> 3, j0 = n & 7;
    const float* red = reinterpret_cast<const float*>(lds + L_RED);
    const int sl = (c >> 2) * 16 + b * 8 + j0;
    float v0 = 0.f, v1 = 0.f;
#pragma unroll
    for (int r = 0; r < 4; ++r) {
      v0 += red[((r * 2 + chh) * 64 + sl) * 4 + (c & 3)];
      v1 += red[((r * 2 + chh) * 64 + sl + 1) * 4 + (c & 3)];
    }
    v4u gr = {__float_as_uint(v0), tag, __float_as_uint(v1), tag};
    __builtin_amdgcn_raw_buffer_store_b128(gr, rs, WS_P2 + ((rb * 2 + b) * 1024 + 256 * cb + n) * 8, 0, AUX_SC1);
  }

  STAMP(8);
  {
    const int b = g & 1, c0 = (g >> 1) * 8;
    const unsigned off = WS_P2 + (((tid >> 3) * 2 + b) * 1024 + c0 + (tid & 7)) * 8;
    const float xres = bf_lo(x[b * DM + c0 + (tid & 7)]);
    v2u pg;
    for (int spin = 0; spin < SPIN_CAP; ++spin) {
      asm volatile("" ::: "memory");
      pg = __builtin_amdgcn_raw_buffer_load_b64(rs, off, 0, AUX_SC1);
      if (__all(pg[1] == tag)) break;
      __builtin_amdgcn_s_sleep(1);
    }
    STAMP(9);
    float v = __uint_as_float(pg[0]);
    v += dpp<ROR8>(v);
    v = x16_32_sum(v);
    float* fin = reinterpret_cast<float*>(lds + L_FIN);
    if (lane < 8) fin[w * 8 + lane] = v;
    BAR();
    if (tid < 8) {
      float s = 0.f;
#pragma unroll
      for (int ww = 0; ww < 8; ++ww) s += fin[ww * 8 + tid];
      out[b * DM + c0 + tid] = (unsigned short)(cvt_pk(xres + s, 0.f) & 0xffffu);
    }
    STAMP(10);
    if (g == 0 && tid == 0) __hip_atomic_store((unsigned*)(ws + WS_EPOCH), tag, __ATOMIC_RELAXED, __HIP_MEMORY_SCOPE_AGENT);
  }
}

}

extern "C" void kernel_launch(void* const* d_in, const int* in_sizes, int n_in, void* d_out, int out_size, void* d_ws, size_t ws_size,
                              hipStream_t stream) {
  (void)in_sizes; (void)n_in; (void)out_size; (void)ws_size;
  hipLaunchKernelGGL(fused_decode, dim3(NWG), dim3(NTHR), 0, stream, (const unsigned short*)d_in[0], (const unsigned short*)d_in[1],
                     (const unsigned short*)d_in[2], (const unsigned short*)d_in[3], (const unsigned short*)d_in[4],
                     (const unsigned short*)d_in[6], (const unsigned short*)d_in[7], (unsigned short*)d_out, (unsigned char*)d_ws);
}
```

```cpp
#include <hip/hip_runtime.h>
#include <hip/hip_bf16.h>
#include <stdint.h>

namespace {

typedef unsigned v4u __attribute__((ext_vector_type(4)));
typedef unsigned v2u __attribute__((ext_vector_type(2)));
typedef float f32x4 __attribute__((ext_vector_type(4)));
typedef short s16x4 __attribute__((ext_vector_type(4)));
typedef __bf16 bf16x8 __attribute__((ext_vector_type(8)));
typedef __bf16 bf16x2 __attribute__((ext_vector_type(2)));

constexpr int DM = 1024;
constexpr int NQ = 4096;
constexpr int SK = 32768;
constexpr int FF = 3584;
constexpr int NWG = 256, NTHR = 512;

constexpr unsigned WS_EPOCH = 0;
constexpr unsigned WS_QG = 4096;
constexpr unsigned WS_SG = WS_QG + 131072;
constexpr unsigned WS_PML = WS_SG + 131072;
constexpr unsigned WS_PO = WS_PML + 32768;
constexpr unsigned WS_P2 = WS_PO + 1024u * 1024;
constexpr unsigned WS_END = WS_P2 + 1024u * 1024;

constexpr int L_RQ = 0;
constexpr int L_Q = 4096;
constexpr int L_ML = 6144;
constexpr int L_WG = 6656;
constexpr int L_OC = 7168;
constexpr int L_RED = 9216;
constexpr int L_OR = L_RED + 16384;
constexpr int OR_W = 4 * 272;
constexpr int L_BYTES = L_OR + 8 * OR_W * 4;

constexpr int AUX_SC1 = 16, AUX_NT = 2;
constexpr int SPIN_CAP = 1 << 16;
#define LATE_THR 56
constexpr float LOG2E = 1.4426950408889634f;
constexpr float QK_SCALE = 0.08838834764831845f;

__device__ __forceinline__ float bf_lo(unsigned u) { return __uint_as_float(u << 16); }
__device__ __forceinline__ float bf_hi(unsigned u) { return __uint_as_float(u & 0xffff0000u); }
__device__ __forceinline__ unsigned cvt_pk(float lo, float hi) {
  bf16x2 p = {(__bf16)lo, (__bf16)hi};
  return __builtin_bit_cast(unsigned, p);
}
__device__ __forceinline__ float rbf(float x) { return bf_lo(cvt_pk(x, x)); }

template <int CTRL> __device__ __forceinline__ float dpp(float x) {
  return __builtin_bit_cast(float, __builtin_amdgcn_mov_dpp(__builtin_bit_cast(int, x), CTRL, 0xf, 0xf, true));
}
constexpr int XOR1 = 0xB1, XOR2 = 0x4E, HMIRROR = 0x141, MIRROR = 0x140, ROR8 = 0x128;
__device__ __forceinline__ float swap16_add(float a, float b) {
  auto s = __builtin_amdgcn_permlane16_swap(__float_as_uint(a), __float_as_uint(b), false, false);
  return __uint_as_float(s[0]) + __uint_as_float(s[1]);
}
__device__ __forceinline__ float swap32_add(float a, float b) {
  auto s = __builtin_amdgcn_permlane32_swap(__float_as_uint(a), __float_as_uint(b), false, false);
  return __uint_as_float(s[0]) + __uint_as_float(s[1]);
}
__device__ __forceinline__ float swap16_max(float a) {
  auto s = __builtin_amdgcn_permlane16_swap(__float_as_uint(a), __float_as_uint(a), false, false);
  return fmaxf(__uint_as_float(s[0]), __uint_as_float(s[1]));
}
__device__ __forceinline__ float swap32_max(float a) {
  auto s = __builtin_amdgcn_permlane32_swap(__float_as_uint(a), __float_as_uint(a), false, false);
  return fmaxf(__uint_as_float(s[0]), __uint_as_float(s[1]));
}
__device__ __forceinline__ float x16_32_sum(float x) { x = swap16_add(x, x); return swap32_add(x, x); }
__device__ __forceinline__ float x16_32_max(float x) { return swap32_max(swap16_max(x)); }
__device__ __forceinline__ float row16_sum(float x) {
  x += dpp<XOR1>(x); x += dpp<XOR2>(x); x += dpp<HMIRROR>(x); x += dpp<MIRROR>(x);
  return x;
}
__device__ __forceinline__ float row16_max(float x) {
  x = fmaxf(x, dpp<XOR1>(x)); x = fmaxf(x, dpp<XOR2>(x)); x = fmaxf(x, dpp<HMIRROR>(x)); x = fmaxf(x, dpp<MIRROR>(x));
  return x;
}
__device__ __forceinline__ float wave_sum(float x) { return x16_32_sum(row16_sum(x)); }

__device__ __forceinline__ v4u ldnt(const void* p) { return __builtin_nontemporal_load((const v4u*)p); }
__device__ __forceinline__ f32x4 mfma32(v4u a, v4u b, f32x4 c) {
  return __builtin_amdgcn_mfma_f32_16x16x32_bf16(__builtin_bit_cast(bf16x8, a), __builtin_bit_cast(bf16x8, b), c, 0, 0, 0);
}
__device__ __forceinline__ f32x4 mfma16(v2u a, v2u b, f32x4 c) {
  return __builtin_amdgcn_mfma_f32_16x16x16bf16_1k(__builtin_bit_cast(s16x4, a), __builtin_bit_cast(s16x4, b), c, 0, 0, 0);
}
#define SB() __builtin_amdgcn_sched_barrier(0)
#define STAMP(i) do { } while (0)
#define BAR() asm volatile("s_waitcnt lgkmcnt(0)\n\ts_barrier" ::: "memory")

__device__ __forceinline__ void slice_reduce(const float (&acc)[16], float* red, int w, int lane) {
  float a8[8], a4[4];
#pragma unroll
  for (int j = 0; j < 8; ++j) a8[j] = swap32_add(acc[j], acc[8 + j]);
#pragma unroll
  for (int j = 0; j < 4; ++j) a4[j] = swap16_add(a8[j], a8[4 + j]);
#pragma unroll
  for (int j = 0; j < 4; ++j) a4[j] += dpp<ROR8>(a4[j]);
  if ((lane & 8) == 0) {
    const int b = lane >> 5, r1 = (lane >> 4) & 1, piece = lane & 7;
    *reinterpret_cast<f32x4*>(red + w * 128 + b * 64 + piece * 8 + r1 * 4) = f32x4{a4[0], a4[1], a4[2], a4[3]};
  }
}

__global__ void __launch_bounds__(NTHR, 2)
fused_decode(const unsigned short* __restrict__ x, const unsigned short* __restrict__ kc, const unsigned short* __restrict__ vc,
             const unsigned short* __restrict__ wq, const unsigned short* __restrict__ wo, const unsigned short* __restrict__ w1,
             const unsigned short* __restrict__ w2, unsigned short* __restrict__ out, unsigned char* ws) {
  __shared__ __attribute__((aligned(16))) unsigned char lds[L_BYTES];
  const int tid = threadIdx.x, lane = tid & 63, w = __builtin_amdgcn_readfirstlane(tid >> 6);
  const int g = blockIdx.x, xc = g & 7, gi = g >> 3;
  const __amdgpu_buffer_rsrc_t rs = __builtin_amdgcn_make_buffer_rsrc(ws, 0, WS_END, 0x00020000);

  STAMP(0);
  const int ab = xc >> 2, kvh = xc & 3, split = gi;
  const size_t kvbase = ((size_t)(ab * 4 + kvh) * SK + (size_t)split * 1024 + 32 * w) * 128;
  const int r16 = lane & 15, gq = lane >> 4;
  const unsigned short* kp = kc + kvbase + (4 * (r16 & 3) + (r16 >> 2)) * 128 + gq * 8;
  const unsigned short* vp = vc + kvbase + lane * 8;
  v4u kr[4][8], vr[4][8];
#define LD_K(c, i_) kr[c][i_] = ldnt(kp + (c) * 256 * 128 + ((i_) >> 2) * 16 * 128 + ((i_) & 3) * 32)
#define ISSUE_K(c) do { _Pragma("unroll") for (int i_ = 0; i_ < 8; ++i_) LD_K(c, i_); SB(); } while (0)
#define ISSUE_V(c) do { _Pragma("unroll") for (int i_ = 0; i_ < 8; ++i_) vr[c][i_] = ldnt(vp + (c) * 256 * 128 + i_ * 512); SB(); } while (0)

  const unsigned epoch = __hip_atomic_load((const unsigned*)(ws + WS_EPOCH), __ATOMIC_RELAXED, __HIP_MEMORY_SCOPE_AGENT);
  unsigned tag;
  {
    const bool is_q = g < 128, has_a = g < 240;
    const int u = is_q ? g : g - 128;
    const int ar = u & 1, ac = u >> 1;
    const int ld = is_q ? NQ : FF;
    const __amdgpu_buffer_rsrc_t rw = __builtin_amdgcn_make_buffer_rsrc((void*)(is_q ? wq : w1), 0, has_a ? DM * ld * 2 : 0, 0x00020000);
    v4u xs[4];
#pragma unroll
    for (int i = 0; i < 4; ++i) xs[i] = *reinterpret_cast<const v4u*>(x + (lane + 64 * i) * 8);
    const int xrow = 512 * ar + 64 * w + lane;
    const unsigned short xr0 = x[xrow], xr1 = x[DM + xrow];
    SB();
    v4u wr[8];
    {
      int off = ((512 * ar + 64 * w + (lane >> 3)) * ld + 64 * ac + 8 * (lane & 7)) * 2;
#pragma unroll
      for (int i = 0; i < 8; ++i) { wr[i] = __builtin_amdgcn_raw_buffer_load_b128(rw, off, 0, AUX_NT); off += 16 * ld; }
    }
    SB();
    float s0 = 0.f, s1 = 0.f;
#pragma unroll
    for (int i = 0; i < 2; ++i)
#pragma unroll
      for (int k = 0; k < 4; ++k) { s0 += bf_lo(xs[i][k]) + bf_hi(xs[i][k]); s1 += bf_lo(xs[2 + i][k]) + bf_hi(xs[2 + i][k]); }
    const float mean0 = wave_sum(s0) * (1.f / DM), mean1 = wave_sum(s1) * (1.f / DM);
    float v0 = 0.f, v1 = 0.f;
#pragma unroll
    for (int i = 0; i < 2; ++i)
#pragma unroll
      for (int k = 0; k < 4; ++k) {
        float d;
        d = bf_lo(xs[i][k]) - mean0; v0 = fmaf(d, d, v0);
        d = bf_hi(xs[i][k]) - mean0; v0 = fmaf(d, d, v0);
        d = bf_lo(xs[2 + i][k]) - mean1; v1 = fmaf(d, d, v1);
        d = bf_hi(xs[2 + i][k]) - mean1; v1 = fmaf(d, d, v1);
      }
    const float var0 = rbf(wave_sum(v0) * (1.f / DM)), var1 = rbf(wave_sum(v1) * (1.f / DM));
    const float m0 = rbf(mean0), m1 = rbf(mean1);
    const float sd0 = rbf(sqrtf(rbf(var0 + 1e-5f))), sd1 = rbf(sqrtf(rbf(var1 + 1e-5f)));
    const unsigned xnp = cvt_pk(rbf(bf_lo(xr0) - m0) / sd0, rbf(bf_lo(xr1) - m1) / sd1);
    unsigned xb[8];
#pragma unroll
    for (int i = 0; i < 8; ++i) xb[i] = __builtin_amdgcn_ds_bpermute((8 * i + (lane >> 3)) * 4, xnp);
    tag = __builtin_amdgcn_readfirstlane(epoch) * 1664525u + 1013904223u;
    SB();
    float acc[16];
#pragma unroll
    for (int j = 0; j < 16; ++j) acc[j] = 0.f;
#pragma unroll
    for (int i = 0; i < 8; ++i) {
      const float x0 = bf_lo(xb[i]), x1 = bf_hi(xb[i]);
#pragma unroll
      for (int k = 0; k < 4; ++k) {
        const float lo = bf_lo(wr[i][k]), hi = bf_hi(wr[i][k]);
        acc[2 * k] = fmaf(x0, lo, acc[2 * k]);
        acc[2 * k + 1] = fmaf(x0, hi, acc[2 * k + 1]);
        acc[8 + 2 * k] = fmaf(x1, lo, acc[8 + 2 * k]);
        acc[8 + 2 * k + 1] = fmaf(x1, hi, acc[8 + 2 * k + 1]);
      }
      SB();
      LD_K(0, i);
      SB();
    }
    float* rq = reinterpret_cast<float*>(lds + L_RQ);
    slice_reduce(acc, rq, w, lane);
    BAR();
    if (w == 0 && has_a) {
      float p0 = 0.f, p1 = 0.f;
#pragma unroll
      for (int ww = 0; ww < 8; ++ww) {
        const float2 v = *reinterpret_cast<const float2*>(rq + ww * 128 + 2 * lane);
        p0 += v.x; p1 += v.y;
      }
      const unsigned off = (is_q ? WS_QG : WS_SG) + (((lane >> 5) * 2 + ar) * ld + 64 * ac + 2 * (lane & 31)) * 8;
      v4u gr = {__float_as_uint(p0), tag, __float_as_uint(p1), tag};
      __builtin_amdgcn_raw_buffer_store_b128(gr, rs, off, 0, AUX_SC1);
    }
    SB();
  }
  STAMP(1);
  ISSUE_V(0);
  ISSUE_K(1);
  STAMP(2);

  const unsigned qoff = WS_QG + (ab * 2 * NQ + kvh * 1024 + 2 * tid) * 8;
  v4u qg[2];
#pragma unroll
  for (int r = 0; r < 2; ++r) qg[r] = __builtin_amdgcn_raw_buffer_load_b128(rs, qoff + r * NQ * 8, 0, AUX_SC1);
  SB();
  asm volatile("s_barrier" ::: "memory");
  SB();
  ISSUE_V(1);
  STAMP(3);
#define QG_OK() (qg[0][1] == tag && qg[0][3] == tag && qg[1][1] == tag && qg[1][3] == tag)
  if (!__all(QG_OK())) {
    for (int spin = 0; spin < SPIN_CAP; ++spin) {
      asm volatile("" ::: "memory");
#pragma unroll
      for (int r = 0; r < 2; ++r) qg[r] = __builtin_amdgcn_raw_buffer_load_b128(rs, qoff + r * NQ * 8, 0, AUX_SC1);
      if (__all(QG_OK())) break;
      __builtin_amdgcn_s_sleep(2);
    }
  }
  reinterpret_cast<unsigned*>(lds + L_Q)[tid] =
      cvt_pk(__uint_as_float(qg[0][0]) + __uint_as_float(qg[1][0]), __uint_as_float(qg[0][2]) + __uint_as_float(qg[1][2]));
  BAR();
  STAMP(4);
  v4u qf[4];
#pragma unroll
  for (int s = 0; s < 4; ++s) qf[s] = *reinterpret_cast<const v4u*>(lds + L_Q + (r16 & 7) * 256 + s * 64 + gq * 16);

  float m_run = -INFINITY, l_run = 0.f;
  f32x4 oacc[4];
#pragma unroll
  for (int a = 0; a < 4; ++a) oacc[a] = f32x4{0.f, 0.f, 0.f, 0.f};
  unsigned pk[8];
  const unsigned pmask = (lane & 8) ? 0xffff0000u : 0x0000ffffu;

#define QK(c) do { \
    f32x4 sc_[2]; \
    _Pragma("unroll") for (int t_ = 0; t_ < 2; ++t_) { \
      sc_[t_] = f32x4{0.f, 0.f, 0.f, 0.f}; \
      _Pragma("unroll") for (int s_ = 0; s_ < 4; ++s_) sc_[t_] = mfma32(kr[c][4 * t_ + s_], qf[s_], sc_[t_]); \
    } \
    float sv_[8]; float mx_ = -INFINITY; \
    _Pragma("unroll") for (int i_ = 0; i_ < 8; ++i_) { sv_[i_] = rbf(rbf(sc_[i_ >> 2][i_ & 3]) * QK_SCALE); mx_ = fmaxf(mx_, sv_[i_]); } \
    mx_ = x16_32_max(mx_); \
    const float mn_ = fmaxf(m_run, mx_); \
    const float al_ = __builtin_amdgcn_exp2f((m_run - mn_) * LOG2E); \
    m_run = mn_; \
    float ps_ = 0.f; \
    _Pragma("unroll") for (int i_ = 0; i_ < 8; ++i_) { \
      const float p_ = __builtin_amdgcn_exp2f((sv_[i_] - mn_) * LOG2E); ps_ += p_; pk[i_] = cvt_pk(p_, p_) & pmask; } \
    l_run = fmaf(l_run, al_, ps_); \
    _Pragma("unroll") for (int a_ = 0; a_ < 4; ++a_) oacc[a_] *= al_; \
  } while (0)
#define PV(c) do { \
    _Pragma("unroll") for (int i_ = 0; i_ < 8; ++i_) { \
      const v2u lo_ = {vr[c][i_][0], vr[c][i_][1]}, hi_ = {vr[c][i_][2], vr[c][i_][3]}; \
      const v2u b0_ = {pk[i_], 0u}, b1_ = {0u, pk[i_]}; \
      oacc[0] = mfma16(lo_, b0_, oacc[0]); oacc[1] = mfma16(lo_, b1_, oacc[1]); \
      oacc[2] = mfma16(hi_, b0_, oacc[2]); oacc[3] = mfma16(hi_, b1_, oacc[3]); \
    } \
  } while (0)

  const int rb = g >> 2, cb = g & 3;
  const int h = rb >> 1, dh = rb & 1;
  v4u wor[4], w2r[4];
  v2u hg[4];

  QK(0); SB(); ISSUE_K(2);
  PV(0); SB(); ISSUE_V(2);
  QK(1); SB(); ISSUE_K(3);
  PV(1); SB(); ISSUE_V(3);
  QK(2); SB();
  const int frow0 = 56 * rb + 7 * w + gq, frow1 = 56 * rb + 7 * w + (gq < 3 ? 4 + gq : 6);
  {
#pragma unroll
    for (int i = 0; i < 4; ++i)
      hg[i] = __builtin_amdgcn_raw_buffer_load_b64(rs, WS_SG + (((r16 >> 3) * 2 + (i & 1)) * FF + ((i >> 1) ? frow1 : frow0)) * 8, 0, AUX_SC1);
    const unsigned short* p = wo + (size_t)(64 * rb + 8 * w + gq) * DM + 256 * cb + 8 * r16;
#pragma unroll
    for (int i = 0; i < 4; ++i) wor[i] = ldnt(p + (size_t)(i >> 1) * 4 * DM + (i & 1) * 128);
    SB();
  }
  PV(2); SB();
  {
#pragma unroll
    for (int i = 0; i < 4; ++i) w2r[i] = ldnt(w2 + (size_t)((i >> 1) ? frow1 : frow0) * DM + 256 * cb + (i & 1) * 128 + 8 * r16);
    SB();
  }
  QK(3); SB();
  PV(3); SB();

  STAMP(5);
  {
    l_run = x16_32_sum(l_run);
    float* orw = reinterpret_cast<float*>(lds + L_OR) + w * OR_W + gq * 272 + r16 * 16;
#pragma unroll
    for (int a = 0; a < 4; ++a) *reinterpret_cast<f32x4*>(orw + 4 * a) = oacc[a];
    if (lane < 8) *reinterpret_cast<float2*>(lds + L_ML + (lane * 8 + w) * 8) = make_float2(m_run, l_run);
  }
  BAR();
  {
    f32x4 ml[4];
#pragma unroll
    for (int i = 0; i < 4; ++i) ml[i] = *reinterpret_cast<const f32x4*>(lds + L_ML + w * 64 + i * 16);
    const float* orr = reinterpret_cast<const float*>(lds + L_OR) + (lane >> 4) * 272 + w * 16 + (lane & 3) * 4 + ((lane >> 2) & 3);
    float oa[8], ob[8];
#pragma unroll
    for (int ww = 0; ww < 8; ++ww) { oa[ww] = orr[ww * OR_W]; ob[ww] = orr[ww * OR_W + 128]; }
    float M = fmaxf(fmaxf(fmaxf(ml[0][0], ml[0][2]), fmaxf(ml[1][0], ml[1][2])), fmaxf(fmaxf(ml[2][0], ml[2][2]), fmaxf(ml[3][0], ml[3][2])));
    float L = 0.f, o0 = 0.f, o1 = 0.f;
#pragma unroll
    for (int ww = 0; ww < 8; ++ww) {
      const float e = __builtin_amdgcn_exp2f((ml[ww >> 1][(ww & 1) * 2] - M) * LOG2E);
      L = fmaf(e, ml[ww >> 1][(ww & 1) * 2 + 1], L);
      o0 = fmaf(e, oa[ww], o0);
      o1 = fmaf(e, ob[ww], o1);
    }
    const unsigned slot = (unsigned)((ab * 32 + kvh * 8 + w) * 32 + split);
    v2u gr = {cvt_pk(o0, o1), tag};
    __builtin_amdgcn_raw_buffer_store_b64(gr, rs, WS_PO + slot * 512 + lane * 8, 0, AUX_SC1);
    if (lane == 0) {
      v4u g2 = {__float_as_uint(M), tag, __float_as_uint(L), tag};
      __builtin_amdgcn_raw_buffer_store_b128(g2, rs, WS_PML + slot * 16, 0, AUX_SC1);
    }
  }

  STAMP(6);
  f32x4 acc[2] = {f32x4{0.f, 0.f, 0.f, 0.f}, f32x4{0.f, 0.f, 0.f, 0.f}};
  {
    constexpr unsigned OOR = 0x7ffffff0u;
    const unsigned slot0 = ((w >> 2) * 32 + h) * 32 + 8 * (w & 3);
    const unsigned pml = lane < 8 ? WS_PML + (slot0 + lane) * 16 : OOR;
    const unsigned po = WS_PO + (slot0 + gq) * 512 + dh * 256 + r16 * 16;
    v4u ml = {0u, ~tag, 0u, ~tag}, og0 = ml, og1 = ml;
    v4u p_m, p_0, p_1;
    p_m = __builtin_amdgcn_raw_buffer_load_b128(rs, pml, 0, AUX_SC1);
    const float xres = bf_lo(x[(g & 1) * DM + (g >> 1) * 8 + (lane >> 5) * 4 + ((lane >> 4) & 1) * 2 + ((lane >> 3) & 1)]);
    SB();
    unsigned mk[4];
#pragma unroll
    for (int r = 0; r < 4; ++r) mk[r] = (r == ((r16 & 7) >> 1)) ? ((r16 & 1) ? 0xffff0000u : 0x0000ffffu) : 0u;
#define VALID(v) (((v)[1] == tag) & ((v)[3] == tag))
#define REQUEST() { \
      const unsigned lm_ = __popc(rdy_) >= 6 ? 0xffu : rdy_; \
      const unsigned a0_ = (!VALID(og0) & (bool)((lm_ >> gq) & 1u)) ? po : OOR; \
      const unsigned a1_ = (!VALID(og1) & (bool)((lm_ >> (gq + 4)) & 1u)) ? po + 4 * 512 : OOR; \
      p_m = __builtin_amdgcn_raw_buffer_load_b128(rs, VALID(ml) ? OOR : pml, 0, AUX_SC1); \
      p_0 = __builtin_amdgcn_raw_buffer_load_b128(rs, a0_, 0, AUX_SC1); \
      p_1 = __builtin_amdgcn_raw_buffer_load_b128(rs, a1_, 0, AUX_SC1); }
    {
      if (VALID(p_m)) ml = p_m;
      const unsigned rdy_ = (unsigned)__ballot(VALID(ml)) & 0xffu;
      REQUEST()
    }
    SB();
    if (!__all(hg[0][1] == tag && hg[1][1] == tag && hg[2][1] == tag && hg[3][1] == tag)) {
      for (int spin = 0; spin < SPIN_CAP; ++spin) {
        asm volatile("" ::: "memory");
#pragma unroll
        for (int i = 0; i < 4; ++i)
          hg[i] = __builtin_amdgcn_raw_buffer_load_b64(rs, WS_SG + (((r16 >> 3) * 2 + (i & 1)) * FF + ((i >> 1) ? frow1 : frow0)) * 8, 0, AUX_SC1);
        if (__all(hg[0][1] == tag && hg[1][1] == tag && hg[2][1] == tag && hg[3][1] == tag)) break;
        __builtin_amdgcn_s_sleep(1);
      }
    }
#pragma unroll
    for (int rg = 0; rg < 2; ++rg) {
      const float hv = rbf(__uint_as_float(hg[2 * rg][0]) + __uint_as_float(hg[2 * rg + 1][0]));
      const float sv = hv / (1.f + __expf(-hv));
      unsigned pkv = cvt_pk(sv, sv);
      if (rg == 1 && gq == 3) pkv = 0u;
      const v4u bf = {pkv & mk[0], pkv & mk[1], pkv & mk[2], pkv & mk[3]};
      acc[0] = mfma32(w2r[2 * rg], bf, acc[0]);
      acc[1] = mfma32(w2r[2 * rg + 1], bf, acc[1]);
    }
    SB();
    STAMP(11);
    for (int spin = 0; spin < SPIN_CAP; ++spin) {
      if (VALID(p_m)) ml = p_m;
      if (VALID(p_0)) og0 = p_0;
      if (VALID(p_1)) og1 = p_1;
      const unsigned rdy_ = (unsigned)__ballot(VALID(ml)) & 0xffu;
      if (rdy_ == 0xffu && __all(VALID(og0) & VALID(og1))) break;
      asm volatile("" ::: "memory");
      REQUEST()
    }
    STAMP(7);
    {
      const float ms = __uint_as_float(ml[0]), ls = __uint_as_float(ml[2]);
      float M = fmaxf(ms, dpp<XOR1>(ms)); M = fmaxf(M, dpp<XOR2>(M)); M = fmaxf(M, dpp<HMIRROR>(M));
      const float e = __builtin_amdgcn_exp2f((ms - M) * LOG2E);
      float L = e * ls; L += dpp<XOR1>(L); L += dpp<XOR2>(L); L += dpp<HMIRROR>(L);
      const float wa = __int_as_float(__builtin_amdgcn_ds_bpermute(gq * 4, __float_as_int(e)));
      const float wb = __int_as_float(__builtin_amdgcn_ds_bpermute((gq + 4) * 4, __float_as_int(e)));
      const float v0 = fmaf(wa, bf_lo(og0[0]), wb * bf_lo(og1[0])), v1 = fmaf(wa, bf_hi(og0[0]), wb * bf_hi(og1[0]));
      const float v2 = fmaf(wa, bf_lo(og0[2]), wb * bf_lo(og1[2])), v3 = fmaf(wa, bf_hi(og0[2]), wb * bf_hi(og1[2]));
      const float t0 = swap32_add(v0, v2), t1 = swap32_add(v1, v3);
      const float o = swap16_add(t0, t1);
      reinterpret_cast<float*>(lds + L_OC)[((w >> 2) * 64 + 4 * r16 + 2 * (lane >> 5) + ((lane >> 4) & 1)) * 4 + (w & 3)] = o;
      if (lane == 0) *reinterpret_cast<float2*>(lds + L_WG + w * 8) = make_float2(M, L);
    }
    BAR();
    {
      const f32x4 a = *reinterpret_cast<const f32x4*>(lds + L_WG + (r16 >> 3) * 32), b = *reinterpret_cast<const f32x4*>(lds + L_WG + (r16 >> 3) * 32 + 16);
      const float M = fmaxf(fmaxf(a[0], a[2]), fmaxf(b[0], b[2]));
      float s0 = __builtin_amdgcn_exp2f((a[0] - M) * LOG2E), s1 = __builtin_amdgcn_exp2f((a[2] - M) * LOG2E);
      float s2 = __builtin_amdgcn_exp2f((b[0] - M) * LOG2E), s3 = __builtin_amdgcn_exp2f((b[2] - M) * LOG2E);
      const float inv = __builtin_amdgcn_rcpf(fmaf(s0, a[1], fmaf(s1, a[3], fmaf(s2, b[1], s3 * b[3]))));
      s0 *= inv; s1 *= inv; s2 *= inv; s3 *= inv;
#pragma unroll
      for (int rg = 0; rg < 2; ++rg) {
        const f32x4 o4 = *reinterpret_cast<const f32x4*>(lds + L_OC + ((r16 >> 3) * 64 + 8 * w + 4 * rg + gq) * 16);
        const float o = fmaf(s0, o4[0], fmaf(s1, o4[1], fmaf(s2, o4[2], s3 * o4[3])));
        const unsigned pkv = cvt_pk(o, o);
        const v4u bf = {pkv & mk[0], pkv & mk[1], pkv & mk[2], pkv & mk[3]};
        acc[0] = mfma32(wor[2 * rg], bf, acc[0]);
        acc[1] = mfma32(wor[2 * rg + 1], bf, acc[1]);
      }
    }
    *reinterpret_cast<f32x4*>(lds + L_RED + (w * 128 + lane) * 16) = acc[0];
    *reinterpret_cast<f32x4*>(lds + L_RED + (w * 128 + 64 + lane) * 16) = acc[1];
    BAR();
    {
      const int b = tid >> 8, n = tid & 255, c = (n & 127) >> 3;
      const float* red = reinterpret_cast<const float*>(lds + L_RED) + ((n >> 7) * 64 + (c >> 2) * 16 + b * 8 + (n & 7)) * 4 + (c & 3);
      float v = 0.f;
#pragma unroll
      for (int ww = 0; ww < 8; ++ww) v += red[ww * 512];
      v2u gr = {__float_as_uint(v), tag};
      __builtin_amdgcn_raw_buffer_store_b64(gr, rs, WS_P2 + ((rb * 2 + b) * 1024 + 256 * cb + n) * 8, 0, AUX_SC1);
    }
    STAMP(8);
    if (w == 0) {
      const int b = g & 1, c0 = (g >> 1) * 8;
      const unsigned off = WS_P2 + ((lane * 2 + b) * 1024 + c0) * 8;
      v4u pg[4];
#pragma unroll
      for (int i = 0; i < 4; ++i) pg[i] = __builtin_amdgcn_raw_buffer_load_b128(rs, off + i * 16, 0, AUX_SC1);
      for (int spin = 0; spin < SPIN_CAP; ++spin) {
        const bool ok = VALID(pg[0]) & VALID(pg[1]) & VALID(pg[2]) & VALID(pg[3]);
        if (__all(ok)) break;
        asm volatile("" ::: "memory");
        if (!ok) {
#pragma unroll
          for (int i = 0; i < 4; ++i) pg[i] = __builtin_amdgcn_raw_buffer_load_b128(rs, off + i * 16, 0, AUX_SC1);
        }
      }
      STAMP(9);
      float u[4], t[2];
#pragma unroll
      for (int j = 0; j < 4; ++j)
        u[j] = swap32_add(__uint_as_float(pg[j >> 1][(j & 1) * 2]), __uint_as_float(pg[2 + (j >> 1)][(j & 1) * 2]));
#pragma unroll
      for (int j = 0; j < 2; ++j) t[j] = swap16_add(u[j], u[j + 2]);
      const float t0 = t[0] + dpp<ROR8>(t[0]), t1 = t[1] + dpp<ROR8>(t[1]);
      float r = (lane & 8) ? t1 : t0;
      r += dpp<HMIRROR>(r); r += dpp<XOR2>(r); r += dpp<XOR1>(r);
      if ((lane & 7) == 0) out[b * DM + c0 + (lane >> 5) * 4 + ((lane >> 4) & 1) * 2 + ((lane >> 3) & 1)] = (unsigned short)(cvt_pk(xres + r, 0.f) & 0xffffu);
      STAMP(10);
      if (g == 0 && lane == 0) __hip_atomic_store((unsigned*)(ws + WS_EPOCH), tag, __ATOMIC_RELAXED, __HIP_MEMORY_SCOPE_AGENT);
    }
  }
}

}

extern "C" void kernel_launch(void* const* d_in, const int* in_sizes, int n_in, void* d_out, int out_size, void* d_ws, size_t ws_size,
                              hipStream_t stream) {
  (void)in_sizes; (void)n_in; (void)out_size; (void)ws_size;
  hipLaunchKernelGGL(fused_decode, dim3(NWG), dim3(NTHR), 0, stream, (const unsigned short*)d_in[0], (const unsigned short*)d_in[1],
                     (const unsigned short*)d_in[2], (const unsigned short*)d_in[3], (const unsigned short*)d_in[4],
                     (const unsigned short*)d_in[6], (const unsigned short*)d_in[7], (unsigned short*)d_out, (unsigned char*)d_ws);
}
```

```cpp
#include <hip/hip_runtime.h>
#include <hip/hip_bf16.h>
#include <stdint.h>

namespace {

typedef unsigned v4u __attribute__((ext_vector_type(4)));
typedef unsigned v2u __attribute__((ext_vector_type(2)));
typedef float f32x4 __attribute__((ext_vector_type(4)));
typedef short s16x4 __attribute__((ext_vector_type(4)));
typedef __bf16 bf16x8 __attribute__((ext_vector_type(8)));
typedef __bf16 bf16x2 __attribute__((ext_vector_type(2)));

constexpr int DM = 1024;
constexpr int NQ = 4096;
constexpr int SK = 32768;
constexpr int FF = 3584;
constexpr int NWG = 256, NTHR = 512;

constexpr unsigned WS_EPOCH = 0;
constexpr unsigned WS_QG = 4096;
constexpr unsigned WS_SG = WS_QG + 131072;
constexpr unsigned WS_PML = WS_SG + 131072;
constexpr unsigned WS_PO = WS_PML + 32768;
constexpr unsigned WS_P2 = WS_PO + 1024u * 1024;
constexpr unsigned WS_END = WS_P2 + 1024u * 1024;

constexpr int L_RQ = 0;
constexpr int L_Q = 4096;
constexpr int L_ML = 6144;
constexpr int L_WG = 6656;
constexpr int L_OC = 7168;
constexpr int L_RED = 9216;
constexpr int L_OR = L_RED + 16384;
constexpr int OR_W = 4 * 272;
constexpr int L_BYTES = L_OR + 8 * OR_W * 4;

constexpr int AUX_SC1 = 16, AUX_NT = 2;
constexpr int SPIN_CAP = 1 << 16;
#define LATE_THR 56
constexpr float LOG2E = 1.4426950408889634f;
constexpr float QK_SCALE = 0.08838834764831845f;

__device__ __forceinline__ float bf_lo(unsigned u) { return __uint_as_float(u << 16); }
__device__ __forceinline__ float bf_hi(unsigned u) { return __uint_as_float(u & 0xffff0000u); }
__device__ __forceinline__ unsigned cvt_pk(float lo, float hi) {
  bf16x2 p = {(__bf16)lo, (__bf16)hi};
  return __builtin_bit_cast(unsigned, p);
}
__device__ __forceinline__ float rbf(float x) { return bf_lo(cvt_pk(x, x)); }

template <int CTRL> __device__ __forceinline__ float dpp(float x) {
  return __builtin_bit_cast(float, __builtin_amdgcn_mov_dpp(__builtin_bit_cast(int, x), CTRL, 0xf, 0xf, true));
}
constexpr int XOR1 = 0xB1, XOR2 = 0x4E, HMIRROR = 0x141, MIRROR = 0x140, ROR8 = 0x128;
__device__ __forceinline__ float swap16_add(float a, float b) {
  auto s = __builtin_amdgcn_permlane16_swap(__float_as_uint(a), __float_as_uint(b), false, false);
  return __uint_as_float(s[0]) + __uint_as_float(s[1]);
}
__device__ __forceinline__ float swap32_add(float a, float b) {
  auto s = __builtin_amdgcn_permlane32_swap(__float_as_uint(a), __float_as_uint(b), false, false);
  return __uint_as_float(s[0]) + __uint_as_float(s[1]);
}
__device__ __forceinline__ float swap16_max(float a) {
  auto s = __builtin_amdgcn_permlane16_swap(__float_as_uint(a), __float_as_uint(a), false, false);
  return fmaxf(__uint_as_float(s[0]), __uint_as_float(s[1]));
}
__device__ __forceinline__ float swap32_max(float a) {
  auto s = __builtin_amdgcn_permlane32_swap(__float_as_uint(a), __float_as_uint(a), false, false);
  return fmaxf(__uint_as_float(s[0]), __uint_as_float(s[1]));
}
__device__ __forceinline__ float x16_32_sum(float x) { x = swap16_add(x, x); return swap32_add(x, x); }
__device__ __forceinline__ float x16_32_max(float x) { return swap32_max(swap16_max(x)); }
__device__ __forceinline__ float row16_sum(float x) {
  x += dpp<XOR1>(x); x += dpp<XOR2>(x); x += dpp<HMIRROR>(x); x += dpp<MIRROR>(x);
  return x;
}
__device__ __forceinline__ float row16_max(float x) {
  x = fmaxf(x, dpp<XOR1>(x)); x = fmaxf(x, dpp<XOR2>(x)); x = fmaxf(x, dpp<HMIRROR>(x)); x = fmaxf(x, dpp<MIRROR>(x));
  return x;
}
__device__ __forceinline__ float wave_sum(float x) { return x16_32_sum(row16_sum(x)); }

__device__ __forceinline__ v4u ldnt(const void* p) { return __builtin_nontemporal_load((const v4u*)p); }
__device__ __forceinline__ f32x4 mfma32(v4u a, v4u b, f32x4 c) {
  return __builtin_amdgcn_mfma_f32_16x16x32_bf16(__builtin_bit_cast(bf16x8, a), __builtin_bit_cast(bf16x8, b), c, 0, 0, 0);
}
__device__ __forceinline__ f32x4 mfma16(v2u a, v2u b, f32x4 c) {
  return __builtin_amdgcn_mfma_f32_16x16x16bf16_1k(__builtin_bit_cast(s16x4, a), __builtin_bit_cast(s16x4, b), c, 0, 0, 0);
}
#define SB() __builtin_amdgcn_sched_barrier(0)
#define STAMP(i) do { } while (0)
#define BAR() asm volatile("s_waitcnt lgkmcnt(0)\n\ts_barrier" ::: "memory")

__device__ __forceinline__ void slice_reduce(const float (&acc)[16], float* red, int w, int lane) {
  float a8[8], a4[4];
#pragma unroll
  for (int j = 0; j < 8; ++j) a8[j] = swap32_add(acc[j], acc[8 + j]);
#pragma unroll
  for (int j = 0; j < 4; ++j) a4[j] = swap16_add(a8[j], a8[4 + j]);
#pragma unroll
  for (int j = 0; j < 4; ++j) a4[j] += dpp<ROR8>(a4[j]);
  if ((lane & 8) == 0) {
    const int b = lane >> 5, r1 = (lane >> 4) & 1, piece = lane & 7;
    *reinterpret_cast<f32x4*>(red + w * 128 + b * 64 + piece * 8 + r1 * 4) = f32x4{a4[0], a4[1], a4[2], a4[3]};
  }
}

__global__ void __launch_bounds__(NTHR, 2)
fused_decode(const unsigned short* __restrict__ x, const unsigned short* __restrict__ kc, const unsigned short* __restrict__ vc,
             const unsigned short* __restrict__ wq, const unsigned short* __restrict__ wo, const unsigned short* __restrict__ w1,
             const unsigned short* __restrict__ w2, unsigned short* __restrict__ out, unsigned char* ws) {
  __shared__ __attribute__((aligned(16))) unsigned char lds[L_BYTES];
  const int tid = threadIdx.x, lane = tid & 63, w = __builtin_amdgcn_readfirstlane(tid >> 6);
  const int g = blockIdx.x, xc = g & 7, gi = g >> 3;
  const __amdgpu_buffer_rsrc_t rs = __builtin_amdgcn_make_buffer_rsrc(ws, 0, WS_END, 0x00020000);

  STAMP(0);
  const int ab = xc >> 2, kvh = xc & 3, split = gi;
  const size_t kvbase = ((size_t)(ab * 4 + kvh) * SK + (size_t)split * 1024 + 32 * w) * 128;
  const int r16 = lane & 15, gq = lane >> 4;
  const unsigned short* kp = kc + kvbase + (4 * (r16 & 3) + (r16 >> 2)) * 128 + gq * 8;
  const unsigned short* vp = vc + kvbase + lane * 8;
  v4u kr[4][8], vr[4][8];
#define LD_K(c, i_) kr[c][i_] = ldnt(kp + (c) * 256 * 128 + ((i_) >> 2) * 16 * 128 + ((i_) & 3) * 32)
#define ISSUE_K(c) do { _Pragma("unroll") for (int i_ = 0; i_ < 8; ++i_) LD_K(c, i_); SB(); } while (0)
#define ISSUE_V(c) do { _Pragma("unroll") for (int i_ = 0; i_ < 8; ++i_) vr[c][i_] = ldnt(vp + (c) * 256 * 128 + i_ * 512); SB(); } while (0)

  const unsigned epoch = __hip_atomic_load((const unsigned*)(ws + WS_EPOCH), __ATOMIC_RELAXED, __HIP_MEMORY_SCOPE_AGENT);
  unsigned tag;
  {
    const bool is_q = g < 128, has_a = g < 240;
    const int u = is_q ? g : g - 128;
    const int ar = u & 1, ac = u >> 1;
    const int ld = is_q ? NQ : FF;
    const __amdgpu_buffer_rsrc_t rw = __builtin_amdgcn_make_buffer_rsrc((void*)(is_q ? wq : w1), 0, has_a ? DM * ld * 2 : 0, 0x00020000);
    v4u xs[4];
#pragma unroll
    for (int i = 0; i < 4; ++i) xs[i] = *reinterpret_cast<const v4u*>(x + (lane + 64 * i) * 8);
    const int xrow = 512 * ar + 64 * w + lane;
    const unsigned short xr0 = x[xrow], xr1 = x[DM + xrow];
    SB();
    v4u wr[8];
    {
      int off = ((512 * ar + 64 * w + (lane >> 3)) * ld + 64 * ac + 8 * (lane & 7)) * 2;
#pragma unroll
      for (int i = 0; i < 8; ++i) { wr[i] = __builtin_amdgcn_raw_buffer_load_b128(rw, off, 0, AUX_NT); off += 16 * ld; }
    }
    SB();
    float s0 = 0.f, s1 = 0.f;
#pragma unroll
    for (int i = 0; i < 2; ++i)
#pragma unroll
      for (int k = 0; k < 4; ++k) { s0 += bf_lo(xs[i][k]) + bf_hi(xs[i][k]); s1 += bf_lo(xs[2 + i][k]) + bf_hi(xs[2 + i][k]); }
    const float mean0 = wave_sum(s0) * (1.f / DM), mean1 = wave_sum(s1) * (1.f / DM);
    float v0 = 0.f, v1 = 0.f;
#pragma unroll
    for (int i = 0; i < 2; ++i)
#pragma unroll
      for (int k = 0; k < 4; ++k) {
        float d;
        d = bf_lo(xs[i][k]) - mean0; v0 = fmaf(d, d, v0);
        d = bf_hi(xs[i][k]) - mean0; v0 = fmaf(d, d, v0);
        d = bf_lo(xs[2 + i][k]) - mean1; v1 = fmaf(d, d, v1);
        d = bf_hi(xs[2 + i][k]) - mean1; v1 = fmaf(d, d, v1);
      }
    const float var0 = rbf(wave_sum(v0) * (1.f / DM)), var1 = rbf(wave_sum(v1) * (1.f / DM));
    const float m0 = rbf(mean0), m1 = rbf(mean1);
    const float sd0 = rbf(sqrtf(rbf(var0 + 1e-5f))), sd1 = rbf(sqrtf(rbf(var1 + 1e-5f)));
    const unsigned xnp = cvt_pk(rbf(bf_lo(xr0) - m0) / sd0, rbf(bf_lo(xr1) - m1) / sd1);
    unsigned xb[8];
#pragma unroll
    for (int i = 0; i < 8; ++i) xb[i] = __builtin_amdgcn_ds_bpermute((8 * i + (lane >> 3)) * 4, xnp);
    tag = __builtin_amdgcn_readfirstlane(epoch) * 1664525u + 1013904223u;
    SB();
    float acc[16];
#pragma unroll
    for (int j = 0; j < 16; ++j) acc[j] = 0.f;
#pragma unroll
    for (int i = 0; i < 8; ++i) {
      const float x0 = bf_lo(xb[i]), x1 = bf_hi(xb[i]);
#pragma unroll
      for (int k = 0; k < 4; ++k) {
        const float lo = bf_lo(wr[i][k]), hi = bf_hi(wr[i][k]);
        acc[2 * k] = fmaf(x0, lo, acc[2 * k]);
        acc[2 * k + 1] = fmaf(x0, hi, acc[2 * k + 1]);
        acc[8 + 2 * k] = fmaf(x1, lo, acc[8 + 2 * k]);
        acc[8 + 2 * k + 1] = fmaf(x1, hi, acc[8 + 2 * k + 1]);
      }
      SB();
      LD_K(0, i);
      SB();
    }
    float* rq = reinterpret_cast<float*>(lds + L_RQ);
    slice_reduce(acc, rq, w, lane);
    BAR();
    if (w == 0 && has_a) {
      float p0 = 0.f, p1 = 0.f;
#pragma unroll
      for (int ww = 0; ww < 8; ++ww) {
        const float2 v = *reinterpret_cast<const float2*>(rq + ww * 128 + 2 * lane);
        p0 += v.x; p1 += v.y;
      }
      const unsigned off = (is_q ? WS_QG : WS_SG) + (((lane >> 5) * 2 + ar) * ld + 64 * ac + 2 * (lane & 31)) * 8;
      v4u gr = {__float_as_uint(p0), tag, __float_as_uint(p1), tag};
      __builtin_amdgcn_raw_buffer_store_b128(gr, rs, off, 0, AUX_SC1);
    }
    SB();
  }
  STAMP(1);
  ISSUE_V(0);
  ISSUE_K(1);
  STAMP(2);

  const unsigned qoff = WS_QG + (ab * 2 * NQ + kvh * 1024 + 2 * tid) * 8;
  v4u qg[2];
#pragma unroll
  for (int r = 0; r < 2; ++r) qg[r] = __builtin_amdgcn_raw_buffer_load_b128(rs, qoff + r * NQ * 8, 0, AUX_SC1);
  SB();
  asm volatile("s_barrier" ::: "memory");
  SB();
  ISSUE_V(1);
  STAMP(3);
#define QG_OK() (qg[0][1] == tag && qg[0][3] == tag && qg[1][1] == tag && qg[1][3] == tag)
  if (!__all(QG_OK())) {
    for (int spin = 0; spin < SPIN_CAP; ++spin) {
      asm volatile("" ::: "memory");
#pragma unroll
      for (int r = 0; r < 2; ++r) qg[r] = __builtin_amdgcn_raw_buffer_load_b128(rs, qoff + r * NQ * 8, 0, AUX_SC1);
      if (__all(QG_OK())) break;
      __builtin_amdgcn_s_sleep(2);
    }
  }
  reinterpret_cast<unsigned*>(lds + L_Q)[tid] =
      cvt_pk(__uint_as_float(qg[0][0]) + __uint_as_float(qg[1][0]), __uint_as_float(qg[0][2]) + __uint_as_float(qg[1][2]));
  BAR();
  STAMP(4);
  v4u qf[4];
#pragma unroll
  for (int s = 0; s < 4; ++s) qf[s] = *reinterpret_cast<const v4u*>(lds + L_Q + (r16 & 7) * 256 + s * 64 + gq * 16);

  float m_run = -INFINITY, l_run = 0.f;
  f32x4 oacc[4];
#pragma unroll
  for (int a = 0; a < 4; ++a) oacc[a] = f32x4{0.f, 0.f, 0.f, 0.f};
  unsigned pk[8];
  const unsigned pmask = (lane & 8) ? 0xffff0000u : 0x0000ffffu;

#define QK(c) do { \
    f32x4 sc_[2]; \
    _Pragma("unroll") for (int t_ = 0; t_ < 2; ++t_) { \
      sc_[t_] = f32x4{0.f, 0.f, 0.f, 0.f}; \
      _Pragma("unroll") for (int s_ = 0; s_ < 4; ++s_) sc_[t_] = mfma32(kr[c][4 * t_ + s_], qf[s_], sc_[t_]); \
    } \
    float sv_[8]; float mx_ = -INFINITY; \
    _Pragma("unroll") for (int i_ = 0; i_ < 8; ++i_) { sv_[i_] = rbf(rbf(sc_[i_ >> 2][i_ & 3]) * QK_SCALE); mx_ = fmaxf(mx_, sv_[i_]); } \
    mx_ = x16_32_max(mx_); \
    const float mn_ = fmaxf(m_run, mx_); \
    const float al_ = __builtin_amdgcn_exp2f((m_run - mn_) * LOG2E); \
    m_run = mn_; \
    float ps_ = 0.f; \
    _Pragma("unroll") for (int i_ = 0; i_ < 8; ++i_) { \
      const float p_ = __builtin_amdgcn_exp2f((sv_[i_] - mn_) * LOG2E); ps_ += p_; pk[i_] = cvt_pk(p_, p_) & pmask; } \
    l_run = fmaf(l_run, al_, ps_); \
    _Pragma("unroll") for (int a_ = 0; a_ < 4; ++a_) oacc[a_] *= al_; \
  } while (0)
#define PV(c) do { \
    _Pragma("unroll") for (int i_ = 0; i_ < 8; ++i_) { \
      const v2u lo_ = {vr[c][i_][0], vr[c][i_][1]}, hi_ = {vr[c][i_][2], vr[c][i_][3]}; \
      const v2u b0_ = {pk[i_], 0u}, b1_ = {0u, pk[i_]}; \
      oacc[0] = mfma16(lo_, b0_, oacc[0]); oacc[1] = mfma16(lo_, b1_, oacc[1]); \
      oacc[2] = mfma16(hi_, b0_, oacc[2]); oacc[3] = mfma16(hi_, b1_, oacc[3]); \
    } \
  } while (0)

  const int rb = g >> 2, cb = g & 3;
  const int h = rb >> 1, dh = rb & 1;
  v4u wor[4], w2r[4];
  v2u hg[4];

  QK(0); SB(); ISSUE_K(2);
  PV(0); SB(); ISSUE_V(2);
  QK(1); SB(); ISSUE_K(3);
  PV(1); SB(); ISSUE_V(3);
  QK(2); SB();
  PV(2); SB();
  QK(3); SB();
  asm volatile("s_barrier" ::: "memory");
  SB();
  const int frow0 = 56 * rb + 7 * w + gq, frow1 = 56 * rb + 7 * w + (gq < 3 ? 4 + gq : 6);
  {
#pragma unroll
    for (int i = 0; i < 4; ++i)
      hg[i] = __builtin_amdgcn_raw_buffer_load_b64(rs, WS_SG + (((r16 >> 3) * 2 + (i & 1)) * FF + ((i >> 1) ? frow1 : frow0)) * 8, 0, AUX_SC1);
    const unsigned short* p = wo + (size_t)(64 * rb + 8 * w + gq) * DM + 256 * cb + 8 * r16;
#pragma unroll
    for (int i = 0; i < 4; ++i) w2r[i] = ldnt(w2 + (size_t)((i >> 1) ? frow1 : frow0) * DM + 256 * cb + (i & 1) * 128 + 8 * r16);
#pragma unroll
    for (int i = 0; i < 4; ++i) wor[i] = ldnt(p + (size_t)(i >> 1) * 4 * DM + (i & 1) * 128);
    SB();
  }
  PV(3); SB();

  STAMP(5);
  {
    l_run = x16_32_sum(l_run);
    float* orw = reinterpret_cast<float*>(lds + L_OR) + w * OR_W + gq * 272 + r16 * 16;
#pragma unroll
    for (int a = 0; a < 4; ++a) *reinterpret_cast<f32x4*>(orw + 4 * a) = oacc[a];
    if (lane < 8) *reinterpret_cast<float2*>(lds + L_ML + (lane * 8 + w) * 8) = make_float2(m_run, l_run);
  }
  BAR();
  {
    f32x4 ml[4];
#pragma unroll
    for (int i = 0; i < 4; ++i) ml[i] = *reinterpret_cast<const f32x4*>(lds + L_ML + w * 64 + i * 16);
    const float* orr = reinterpret_cast<const float*>(lds + L_OR) + (lane >> 4) * 272 + w * 16 + (lane & 3) * 4 + ((lane >> 2) & 3);
    float oa[8], ob[8];
#pragma unroll
    for (int ww = 0; ww < 8; ++ww) { oa[ww] = orr[ww * OR_W]; ob[ww] = orr[ww * OR_W + 128]; }
    float M = fmaxf(fmaxf(fmaxf(ml[0][0], ml[0][2]), fmaxf(ml[1][0], ml[1][2])), fmaxf(fmaxf(ml[2][0], ml[2][2]), fmaxf(ml[3][0], ml[3][2])));
    float L = 0.f, o0 = 0.f, o1 = 0.f;
#pragma unroll
    for (int ww = 0; ww < 8; ++ww) {
      const float e = __builtin_amdgcn_exp2f((ml[ww >> 1][(ww & 1) * 2] - M) * LOG2E);
      L = fmaf(e, ml[ww >> 1][(ww & 1) * 2 + 1], L);
      o0 = fmaf(e, oa[ww], o0);
      o1 = fmaf(e, ob[ww], o1);
    }
    const unsigned slot = (unsigned)((ab * 32 + kvh * 8 + w) * 32 + split);
    v2u gr = {cvt_pk(o0, o1), tag};
    __builtin_amdgcn_raw_buffer_store_b64(gr, rs, WS_PO + slot * 512 + lane * 8, 0, AUX_SC1);
    if (lane == 0) {
      v4u g2 = {__float_as_uint(M), tag, __float_as_uint(L), tag};
      __builtin_amdgcn_raw_buffer_store_b128(g2, rs, WS_PML + slot * 16, 0, AUX_SC1);
    }
  }

  STAMP(6);
  f32x4 acc[2] = {f32x4{0.f, 0.f, 0.f, 0.f}, f32x4{0.f, 0.f, 0.f, 0.f}};
  {
    constexpr unsigned OOR = 0x7ffffff0u;
    const unsigned slot0 = ((w >> 2) * 32 + h) * 32 + 8 * (w & 3);
    const unsigned pml = lane < 8 ? WS_PML + (slot0 + lane) * 16 : OOR;
    const unsigned po = WS_PO + (slot0 + gq) * 512 + dh * 256 + r16 * 16;
    v4u ml = {0u, ~tag, 0u, ~tag}, og0 = ml, og1 = ml;
    v4u p_m, p_0, p_1;
    p_m = __builtin_amdgcn_raw_buffer_load_b128(rs, pml, 0, AUX_SC1);
    const float xres = bf_lo(x[(g & 1) * DM + ((g >> 1) & 63) * 16 + 2 * (lane & 7) + (lane >> 5)]);
    SB();
    unsigned mk[4];
#pragma unroll
    for (int r = 0; r < 4; ++r) mk[r] = (r == ((r16 & 7) >> 1)) ? ((r16 & 1) ? 0xffff0000u : 0x0000ffffu) : 0u;
#define VALID(v) (((v)[1] == tag) & ((v)[3] == tag))
#define REQUEST() { \
      const unsigned lm_ = __popc(rdy_) >= 6 ? 0xffu : rdy_; \
      const unsigned a0_ = (!VALID(og0) & (bool)((lm_ >> gq) & 1u)) ? po : OOR; \
      const unsigned a1_ = (!VALID(og1) & (bool)((lm_ >> (gq + 4)) & 1u)) ? po + 4 * 512 : OOR; \
      p_m = __builtin_amdgcn_raw_buffer_load_b128(rs, VALID(ml) ? OOR : pml, 0, AUX_SC1); \
      p_0 = __builtin_amdgcn_raw_buffer_load_b128(rs, a0_, 0, AUX_SC1); \
      p_1 = __builtin_amdgcn_raw_buffer_load_b128(rs, a1_, 0, AUX_SC1); }
    {
      if (VALID(p_m)) ml = p_m;
      const unsigned rdy_ = (unsigned)__ballot(VALID(ml)) & 0xffu;
      REQUEST()
    }
    SB();
    if (!__all(hg[0][1] == tag && hg[1][1] == tag && hg[2][1] == tag && hg[3][1] == tag)) {
      for (int spin = 0; spin < SPIN_CAP; ++spin) {
        asm volatile("" ::: "memory");
#pragma unroll
        for (int i = 0; i < 4; ++i)
          hg[i] = __builtin_amdgcn_raw_buffer_load_b64(rs, WS_SG + (((r16 >> 3) * 2 + (i & 1)) * FF + ((i >> 1) ? frow1 : frow0)) * 8, 0, AUX_SC1);
        if (__all(hg[0][1] == tag && hg[1][1] == tag && hg[2][1] == tag && hg[3][1] == tag)) break;
        __builtin_amdgcn_s_sleep(1);
      }
    }
#pragma unroll
    for (int rg = 0; rg < 2; ++rg) {
      const float hv = rbf(__uint_as_float(hg[2 * rg][0]) + __uint_as_float(hg[2 * rg + 1][0]));
      const float sv = hv / (1.f + __expf(-hv));
      unsigned pkv = cvt_pk(sv, sv);
      if (rg == 1 && gq == 3) pkv = 0u;
      const v4u bf = {pkv & mk[0], pkv & mk[1], pkv & mk[2], pkv & mk[3]};
      acc[0] = mfma32(w2r[2 * rg], bf, acc[0]);
      acc[1] = mfma32(w2r[2 * rg + 1], bf, acc[1]);
    }
    SB();
    STAMP(11);
    for (int spin = 0; spin < SPIN_CAP; ++spin) {
      if (VALID(p_m)) ml = p_m;
      if (VALID(p_0)) og0 = p_0;
      if (VALID(p_1)) og1 = p_1;
      const unsigned rdy_ = (unsigned)__ballot(VALID(ml)) & 0xffu;
      if (rdy_ == 0xffu && __all(VALID(og0) & VALID(og1))) break;
      asm volatile("" ::: "memory");
      REQUEST()
    }
    STAMP(7);
    {
      const float ms = __uint_as_float(ml[0]), ls = __uint_as_float(ml[2]);
      float M = fmaxf(ms, dpp<XOR1>(ms)); M = fmaxf(M, dpp<XOR2>(M)); M = fmaxf(M, dpp<HMIRROR>(M));
      const float e = __builtin_amdgcn_exp2f((ms - M) * LOG2E);
      float L = e * ls; L += dpp<XOR1>(L); L += dpp<XOR2>(L); L += dpp<HMIRROR>(L);
      const float wa = __int_as_float(__builtin_amdgcn_ds_bpermute(gq * 4, __float_as_int(e)));
      const float wb = __int_as_float(__builtin_amdgcn_ds_bpermute((gq + 4) * 4, __float_as_int(e)));
      const float v0 = fmaf(wa, bf_lo(og0[0]), wb * bf_lo(og1[0])), v1 = fmaf(wa, bf_hi(og0[0]), wb * bf_hi(og1[0]));
      const float v2 = fmaf(wa, bf_lo(og0[2]), wb * bf_lo(og1[2])), v3 = fmaf(wa, bf_hi(og0[2]), wb * bf_hi(og1[2]));
      const float t0 = swap32_add(v0, v2), t1 = swap32_add(v1, v3);
      const float o = swap16_add(t0, t1);
      reinterpret_cast<float*>(lds + L_OC)[((w >> 2) * 64 + 4 * r16 + 2 * (lane >> 5) + ((lane >> 4) & 1)) * 4 + (w & 3)] = o;
      if (lane == 0) *reinterpret_cast<float2*>(lds + L_WG + w * 8) = make_float2(M, L);
    }
    BAR();
    {
      const f32x4 a = *reinterpret_cast<const f32x4*>(lds + L_WG + (r16 >> 3) * 32), b = *reinterpret_cast<const f32x4*>(lds + L_WG + (r16 >> 3) * 32 + 16);
      const float M = fmaxf(fmaxf(a[0], a[2]), fmaxf(b[0], b[2]));
      float s0 = __builtin_amdgcn_exp2f((a[0] - M) * LOG2E), s1 = __builtin_amdgcn_exp2f((a[2] - M) * LOG2E);
      float s2 = __builtin_amdgcn_exp2f((b[0] - M) * LOG2E), s3 = __builtin_amdgcn_exp2f((b[2] - M) * LOG2E);
      const float inv = __builtin_amdgcn_rcpf(fmaf(s0, a[1], fmaf(s1, a[3], fmaf(s2, b[1], s3 * b[3]))));
      s0 *= inv; s1 *= inv; s2 *= inv; s3 *= inv;
#pragma unroll
      for (int rg = 0; rg < 2; ++rg) {
        const f32x4 o4 = *reinterpret_cast<const f32x4*>(lds + L_OC + ((r16 >> 3) * 64 + 8 * w + 4 * rg + gq) * 16);
        const float o = fmaf(s0, o4[0], fmaf(s1, o4[1], fmaf(s2, o4[2], s3 * o4[3])));
        const unsigned pkv = cvt_pk(o, o);
        const v4u bf = {pkv & mk[0], pkv & mk[1], pkv & mk[2], pkv & mk[3]};
        acc[0] = mfma32(wor[2 * rg], bf, acc[0]);
        acc[1] = mfma32(wor[2 * rg + 1], bf, acc[1]);
      }
    }
    *reinterpret_cast<f32x4*>(lds + L_RED + (w * 128 + lane) * 16) = acc[0];
    *reinterpret_cast<f32x4*>(lds + L_RED + (w * 128 + 64 + lane) * 16) = acc[1];
    BAR();
    if (tid < 256) {
      const int b = tid >> 7, n = (tid & 127) * 2, c = (n & 127) >> 3;
      const float* red = reinterpret_cast<const float*>(lds + L_RED) + ((n >> 7) * 64 + (c >> 2) * 16 + b * 8 + (n & 7)) * 4 + (c & 3);
      float v0 = 0.f, v1 = 0.f;
#pragma unroll
      for (int ww = 0; ww < 8; ++ww) { v0 += red[ww * 512]; v1 += red[ww * 512 + 4]; }
      v4u gr = {__float_as_uint(v0), tag, __float_as_uint(v1), tag};
      __builtin_amdgcn_raw_buffer_store_b128(gr, rs, WS_P2 + ((rb * 2 + b) * 1024 + 256 * cb + n) * 8, 0, AUX_SC1);
    }
    STAMP(8);
    if (w == 0 && g < 128) {
      const int b = g & 1, c0 = (g >> 1) * 16;
      const unsigned off = WS_P2 + (((lane >> 3) * 2 + b) * 1024 + c0) * 8 + (lane & 7) * 16;
      v4u pg[8];
#pragma unroll
      for (int i = 0; i < 8; ++i) pg[i] = __builtin_amdgcn_raw_buffer_load_b128(rs, off + i * (8 * 2 * 1024 * 8), 0, AUX_SC1);
      for (int spin = 0; spin < SPIN_CAP; ++spin) {
        bool ok = true;
#pragma unroll
        for (int i = 0; i < 8; ++i) ok &= VALID(pg[i]);
        if (__all(ok)) break;
        asm volatile("" ::: "memory");
#pragma unroll
        for (int i = 0; i < 8; ++i) {
          const bool vi = VALID(pg[i]);
          const v4u nw = __builtin_amdgcn_raw_buffer_load_b128(rs, vi ? OOR : off + i * (8 * 2 * 1024 * 8), 0, AUX_SC1);
          if (!vi) pg[i] = nw;
        }
      }
      STAMP(9);
      float v0 = 0.f, v1 = 0.f;
#pragma unroll
      for (int i = 0; i < 8; ++i) { v0 += __uint_as_float(pg[i][0]); v1 += __uint_as_float(pg[i][2]); }
      float r = swap32_add(v0, v1);
      r = swap16_add(r, r);
      r += dpp<ROR8>(r);
      if ((lane & 24) == 0) out[b * DM + c0 + 2 * (lane & 7) + (lane >> 5)] = (unsigned short)(cvt_pk(xres + r, 0.f) & 0xffffu);
      STAMP(10);
      if (g == 0 && lane == 0) __hip_atomic_store((unsigned*)(ws + WS_EPOCH), tag, __ATOMIC_RELAXED, __HIP_MEMORY_SCOPE_AGENT);
    }
  }
}

}

extern "C" void kernel_launch(void* const* d_in, const int* in_sizes, int n_in, void* d_out, int out_size, void* d_ws, size_t ws_size,
                              hipStream_t stream) {
  (void)in_sizes; (void)n_in; (void)out_size; (void)ws_size;
  hipLaunchKernelGGL(fused_decode, dim3(NWG), dim3(NTHR), 0, stream, (const unsigned short*)d_in[0], (const unsigned short*)d_in[1],
                     (const unsigned short*)d_in[2], (const unsigned short*)d_in[3], (const unsigned short*)d_in[4],
                     (const unsigned short*)d_in[6], (const unsigned short*)d_in[7], (unsigned short*)d_out, (unsigned char*)d_ws);
}
```

```cpp
#include <hip/hip_runtime.h>
#include <hip/hip_bf16.h>
#include <stdint.h>

namespace {

typedef unsigned v4u __attribute__((ext_vector_type(4)));
typedef unsigned v2u __attribute__((ext_vector_type(2)));
typedef float f32x4 __attribute__((ext_vector_type(4)));
typedef short s16x4 __attribute__((ext_vector_type(4)));
typedef __bf16 bf16x8 __attribute__((ext_vector_type(8)));
typedef __bf16 bf16x2 __attribute__((ext_vector_type(2)));

constexpr int DM = 1024;
constexpr int NQ = 4096;
constexpr int SK = 32768;
constexpr int FF = 3584;
constexpr int NWG = 256, NTHR = 512;

constexpr unsigned WS_EPOCH = 0;
constexpr unsigned WS_QG = 4096;
constexpr unsigned WS_SG = WS_QG + 131072;
constexpr unsigned WS_PML = WS_SG + 131072;
constexpr unsigned WS_PO = WS_PML + 32768;
constexpr unsigned WS_P2 = WS_PO + 1024u * 1024;
constexpr unsigned WS_END = WS_P2 + 1024u * 1024;

constexpr int L_RQ = 0;
constexpr int L_Q = 4096;
constexpr int L_ML = 6144;
constexpr int L_WG = 6656;
constexpr int L_OC = 7168;
constexpr int L_RED = 9216;
constexpr int L_OR = L_RED + 16384;
constexpr int OR_W = 4 * 272;
constexpr int L_BYTES = L_OR + 8 * OR_W * 4;

constexpr int AUX_SC1 = 16, AUX_NT = 2;
constexpr int SPIN_CAP = 1 << 16;
constexpr float LOG2E = 1.4426950408889634f;
constexpr float QK_SCALE = 0.08838834764831845f;

__device__ __forceinline__ float bf_lo(unsigned u) { return __uint_as_float(u << 16); }
__device__ __forceinline__ float bf_hi(unsigned u) { return __uint_as_float(u & 0xffff0000u); }
__device__ __forceinline__ unsigned cvt_pk(float lo, float hi) {
  bf16x2 p = {(__bf16)lo, (__bf16)hi};
  return __builtin_bit_cast(unsigned, p);
}
__device__ __forceinline__ float rbf(float x) { return bf_lo(cvt_pk(x, x)); }

template <int CTRL> __device__ __forceinline__ float dpp(float x) {
  return __builtin_bit_cast(float, __builtin_amdgcn_mov_dpp(__builtin_bit_cast(int, x), CTRL, 0xf, 0xf, true));
}
constexpr int XOR1 = 0xB1, XOR2 = 0x4E, HMIRROR = 0x141, MIRROR = 0x140, ROR8 = 0x128;
__device__ __forceinline__ float swap16_add(float a, float b) {
  auto s = __builtin_amdgcn_permlane16_swap(__float_as_uint(a), __float_as_uint(b), false, false);
  return __uint_as_float(s[0]) + __uint_as_float(s[1]);
}
__device__ __forceinline__ float swap32_add(float a, float b) {
  auto s = __builtin_amdgcn_permlane32_swap(__float_as_uint(a), __float_as_uint(b), false, false);
  return __uint_as_float(s[0]) + __uint_as_float(s[1]);
}
__device__ __forceinline__ float swap16_max(float a) {
  auto s = __builtin_amdgcn_permlane16_swap(__float_as_uint(a), __float_as_uint(a), false, false);
  return fmaxf(__uint_as_float(s[0]), __uint_as_float(s[1]));
}
__device__ __forceinline__ float swap32_max(float a) {
  auto s = __builtin_amdgcn_permlane32_swap(__float_as_uint(a), __float_as_uint(a), false, false);
  return fmaxf(__uint_as_float(s[0]), __uint_as_float(s[1]));
}
__device__ __forceinline__ float x16_32_sum(float x) { x = swap16_add(x, x); return swap32_add(x, x); }
__device__ __forceinline__ float x16_32_max(float x) { return swap32_max(swap16_max(x)); }
__device__ __forceinline__ float row16_sum(float x) {
  x += dpp<XOR1>(x); x += dpp<XOR2>(x); x += dpp<HMIRROR>(x); x += dpp<MIRROR>(x);
  return x;
}
__device__ __forceinline__ float wave_sum(float x) { return x16_32_sum(row16_sum(x)); }

__device__ __forceinline__ v4u ldnt(const void* p) { return __builtin_nontemporal_load((const v4u*)p); }
__device__ __forceinline__ f32x4 mfma32(v4u a, v4u b, f32x4 c) {
  return __builtin_amdgcn_mfma_f32_16x16x32_bf16(__builtin_bit_cast(bf16x8, a), __builtin_bit_cast(bf16x8, b), c, 0, 0, 0);
}
__device__ __forceinline__ f32x4 mfma16(v2u a, v2u b, f32x4 c) {
  return __builtin_amdgcn_mfma_f32_16x16x16bf16_1k(__builtin_bit_cast(s16x4, a), __builtin_bit_cast(s16x4, b), c, 0, 0, 0);
}
#define SB() __builtin_amdgcn_sched_barrier(0)
#define BAR() asm volatile("s_waitcnt lgkmcnt(0)\n\ts_barrier" ::: "memory")

__device__ __forceinline__ void slice_reduce(const float (&acc)[16], float* red, int w, int lane) {
  float a8[8], a4[4];
#pragma unroll
  for (int j = 0; j < 8; ++j) a8[j] = swap32_add(acc[j], acc[8 + j]);
#pragma unroll
  for (int j = 0; j < 4; ++j) a4[j] = swap16_add(a8[j], a8[4 + j]);
#pragma unroll
  for (int j = 0; j < 4; ++j) a4[j] += dpp<ROR8>(a4[j]);
  if ((lane & 8) == 0) {
    const int b = lane >> 5, r1 = (lane >> 4) & 1, piece = lane & 7;
    *reinterpret_cast<f32x4*>(red + w * 128 + b * 64 + piece * 8 + r1 * 4) = f32x4{a4[0], a4[1], a4[2], a4[3]};
  }
}

__global__ void __launch_bounds__(NTHR, 2)
fused_decode(const unsigned short* __restrict__ x, const unsigned short* __restrict__ kc, const unsigned short* __restrict__ vc,
             const unsigned short* __restrict__ wq, const unsigned short* __restrict__ wo, const unsigned short* __restrict__ w1,
             const unsigned short* __restrict__ w2, unsigned short* __restrict__ out, unsigned char* ws) {
  __shared__ __attribute__((aligned(16))) unsigned char lds[L_BYTES];
  const int tid = threadIdx.x, lane = tid & 63, w = __builtin_amdgcn_readfirstlane(tid >> 6);
  const int g = blockIdx.x, xc = g & 7, gi = g >> 3;
  const __amdgpu_buffer_rsrc_t rs = __builtin_amdgcn_make_buffer_rsrc(ws, 0, WS_END, 0x00020000);
  const int ab = xc >> 2, kvh = xc & 3, split = gi;
  const size_t kvbase = ((size_t)(ab * 4 + kvh) * SK + (size_t)split * 1024 + 32 * w) * 128;
  const int r16 = lane & 15, gq = lane >> 4;
  const unsigned short* kp = kc + kvbase + (4 * (r16 & 3) + (r16 >> 2)) * 128 + gq * 8;
  const unsigned short* vp = vc + kvbase + lane * 8;
  v4u kr[4][8], vr[4][8];
#define LD_K(c, i_) kr[c][i_] = ldnt(kp + (c) * 256 * 128 + ((i_) >> 2) * 16 * 128 + ((i_) & 3) * 32)
#define ISSUE_K(c) do { _Pragma("unroll") for (int i_ = 0; i_ < 8; ++i_) LD_K(c, i_); SB(); } while (0)
#define ISSUE_V(c) do { _Pragma("unroll") for (int i_ = 0; i_ < 8; ++i_) vr[c][i_] = ldnt(vp + (c) * 256 * 128 + i_ * 512); SB(); } while (0)

  const unsigned epoch = __hip_atomic_load((const unsigned*)(ws + WS_EPOCH), __ATOMIC_RELAXED, __HIP_MEMORY_SCOPE_AGENT);
  unsigned tag;
  {
    const bool is_q = g < 128, has_a = g < 240;
    const int u = is_q ? g : g - 128;
    const int ar = u & 1, ac = u >> 1;
    const int ld = is_q ? NQ : FF;
    const __amdgpu_buffer_rsrc_t rw = __builtin_amdgcn_make_buffer_rsrc((void*)(is_q ? wq : w1), 0, has_a ? DM * ld * 2 : 0, 0x00020000);
    v4u xs[4];
#pragma unroll
    for (int i = 0; i < 4; ++i) xs[i] = *reinterpret_cast<const v4u*>(x + (lane + 64 * i) * 8);
    const int xrow = 512 * ar + 64 * w + lane;
    const unsigned short xr0 = x[xrow], xr1 = x[DM + xrow];
    SB();
    v4u wr[8];
    {
      int off = ((512 * ar + 64 * w + (lane >> 3)) * ld + 64 * ac + 8 * (lane & 7)) * 2;
#pragma unroll
      for (int i = 0; i < 8; ++i) { wr[i] = __builtin_amdgcn_raw_buffer_load_b128(rw, off, 0, AUX_NT); off += 16 * ld; }
    }
    SB();
    float s0 = 0.f, s1 = 0.f;
#pragma unroll
    for (int i = 0; i < 2; ++i)
#pragma unroll
      for (int k = 0; k < 4; ++k) { s0 += bf_lo(xs[i][k]) + bf_hi(xs[i][k]); s1 += bf_lo(xs[2 + i][k]) + bf_hi(xs[2 + i][k]); }
    const float mean0 = wave_sum(s0) * (1.f / DM), mean1 = wave_sum(s1) * (1.f / DM);
    float v0 = 0.f, v1 = 0.f;
#pragma unroll
    for (int i = 0; i < 2; ++i)
#pragma unroll
      for (int k = 0; k < 4; ++k) {
        float d;
        d = bf_lo(xs[i][k]) - mean0; v0 = fmaf(d, d, v0);
        d = bf_hi(xs[i][k]) - mean0; v0 = fmaf(d, d, v0);
        d = bf_lo(xs[2 + i][k]) - mean1; v1 = fmaf(d, d, v1);
        d = bf_hi(xs[2 + i][k]) - mean1; v1 = fmaf(d, d, v1);
      }
    const float var0 = rbf(wave_sum(v0) * (1.f / DM)), var1 = rbf(wave_sum(v1) * (1.f / DM));
    const float m0 = rbf(mean0), m1 = rbf(mean1);
    const float sd0 = rbf(sqrtf(rbf(var0 + 1e-5f))), sd1 = rbf(sqrtf(rbf(var1 + 1e-5f)));
    const unsigned xnp = cvt_pk(rbf(bf_lo(xr0) - m0) / sd0, rbf(bf_lo(xr1) - m1) / sd1);
    unsigned xb[8];
#pragma unroll
    for (int i = 0; i < 8; ++i) xb[i] = __builtin_amdgcn_ds_bpermute((8 * i + (lane >> 3)) * 4, xnp);
    tag = __builtin_amdgcn_readfirstlane(epoch) * 1664525u + 1013904223u;
    SB();
    float acc[16];
#pragma unroll
    for (int j = 0; j < 16; ++j) acc[j] = 0.f;
#pragma unroll
    for (int i = 0; i < 8; ++i) {
      const float x0 = bf_lo(xb[i]), x1 = bf_hi(xb[i]);
#pragma unroll
      for (int k = 0; k < 4; ++k) {
        const float lo = bf_lo(wr[i][k]), hi = bf_hi(wr[i][k]);
        acc[2 * k] = fmaf(x0, lo, acc[2 * k]);
        acc[2 * k + 1] = fmaf(x0, hi, acc[2 * k + 1]);
        acc[8 + 2 * k] = fmaf(x1, lo, acc[8 + 2 * k]);
        acc[8 + 2 * k + 1] = fmaf(x1, hi, acc[8 + 2 * k + 1]);
      }
      SB();
      LD_K(0, i);
      SB();
    }
    float* rq = reinterpret_cast<float*>(lds + L_RQ);
    slice_reduce(acc, rq, w, lane);
    BAR();
    if (w == 0 && has_a) {
      float p0 = 0.f, p1 = 0.f;
#pragma unroll
      for (int ww = 0; ww < 8; ++ww) {
        const float2 v = *reinterpret_cast<const float2*>(rq + ww * 128 + 2 * lane);
        p0 += v.x; p1 += v.y;
      }
      const unsigned off = (is_q ? WS_QG : WS_SG) + (((lane >> 5) * 2 + ar) * ld + 64 * ac + 2 * (lane & 31)) * 8;
      v4u gr = {__float_as_uint(p0), tag, __float_as_uint(p1), tag};
      __builtin_amdgcn_raw_buffer_store_b128(gr, rs, off, 0, AUX_SC1);
    }
    SB();
  }
  ISSUE_V(0);
  ISSUE_K(1);

  const unsigned qoff = WS_QG + (ab * 2 * NQ + kvh * 1024 + 2 * tid) * 8;
  v4u qg[2];
#pragma unroll
  for (int r = 0; r < 2; ++r) qg[r] = __builtin_amdgcn_raw_buffer_load_b128(rs, qoff + r * NQ * 8, 0, AUX_SC1);
  SB();
  asm volatile("s_barrier" ::: "memory");
  SB();
  ISSUE_V(1);
#define QG_OK() (qg[0][1] == tag && qg[0][3] == tag && qg[1][1] == tag && qg[1][3] == tag)
  if (!__all(QG_OK())) {
    for (int spin = 0; spin < SPIN_CAP; ++spin) {
      asm volatile("" ::: "memory");
#pragma unroll
      for (int r = 0; r < 2; ++r) qg[r] = __builtin_amdgcn_raw_buffer_load_b128(rs, qoff + r * NQ * 8, 0, AUX_SC1);
      if (__all(QG_OK())) break;
      __builtin_amdgcn_s_sleep(2);
    }
  }
  reinterpret_cast<unsigned*>(lds + L_Q)[tid] =
      cvt_pk(__uint_as_float(qg[0][0]) + __uint_as_float(qg[1][0]), __uint_as_float(qg[0][2]) + __uint_as_float(qg[1][2]));
  BAR();
  v4u qf[4];
#pragma unroll
  for (int s = 0; s < 4; ++s) qf[s] = *reinterpret_cast<const v4u*>(lds + L_Q + (r16 & 7) * 256 + s * 64 + gq * 16);

  float m_run = -INFINITY, l_run = 0.f;
  f32x4 oacc[4];
#pragma unroll
  for (int a = 0; a < 4; ++a) oacc[a] = f32x4{0.f, 0.f, 0.f, 0.f};
  unsigned pk[8];
  const unsigned pmask = (lane & 8) ? 0xffff0000u : 0x0000ffffu;

#define QK(c) do { \
    f32x4 sc_[2]; \
    _Pragma("unroll") for (int t_ = 0; t_ < 2; ++t_) { \
      sc_[t_] = f32x4{0.f, 0.f, 0.f, 0.f}; \
      _Pragma("unroll") for (int s_ = 0; s_ < 4; ++s_) sc_[t_] = mfma32(kr[c][4 * t_ + s_], qf[s_], sc_[t_]); \
    } \
    float sv_[8]; float mx_ = -INFINITY; \
    _Pragma("unroll") for (int i_ = 0; i_ < 8; ++i_) { sv_[i_] = rbf(rbf(sc_[i_ >> 2][i_ & 3]) * QK_SCALE); mx_ = fmaxf(mx_, sv_[i_]); } \
    mx_ = x16_32_max(mx_); \
    const float mn_ = fmaxf(m_run, mx_); \
    const float al_ = __builtin_amdgcn_exp2f((m_run - mn_) * LOG2E); \
    m_run = mn_; \
    float ps_ = 0.f; \
    _Pragma("unroll") for (int i_ = 0; i_ < 8; ++i_) { \
      const float p_ = __builtin_amdgcn_exp2f((sv_[i_] - mn_) * LOG2E); ps_ += p_; pk[i_] = cvt_pk(p_, p_) & pmask; } \
    l_run = fmaf(l_run, al_, ps_); \
    _Pragma("unroll") for (int a_ = 0; a_ < 4; ++a_) oacc[a_] *= al_; \
  } while (0)
#define PV(c) do { \
    _Pragma("unroll") for (int i_ = 0; i_ < 8; ++i_) { \
      const v2u lo_ = {vr[c][i_][0], vr[c][i_][1]}, hi_ = {vr[c][i_][2], vr[c][i_][3]}; \
      const v2u b0_ = {pk[i_], 0u}, b1_ = {0u, pk[i_]}; \
      oacc[0] = mfma16(lo_, b0_, oacc[0]); oacc[1] = mfma16(lo_, b1_, oacc[1]); \
      oacc[2] = mfma16(hi_, b0_, oacc[2]); oacc[3] = mfma16(hi_, b1_, oacc[3]); \
    } \
  } while (0)

  const int rb = g >> 2, cb = g & 3;
  const int h = rb >> 1, dh = rb & 1;
  v4u wor[4], w2r[4];
  v2u hg[4];

  QK(0); SB(); ISSUE_K(2);
  PV(0); SB(); ISSUE_V(2);
  QK(1); SB(); ISSUE_K(3);
  PV(1); SB(); ISSUE_V(3);
  QK(2); SB();
  PV(2); SB();
  QK(3); SB();
  asm volatile("s_barrier" ::: "memory");
  SB();
  const int frow0 = 56 * rb + 7 * w + gq, frow1 = 56 * rb + 7 * w + (gq < 3 ? 4 + gq : 6);
  {
#pragma unroll
    for (int i = 0; i < 4; ++i)
      hg[i] = __builtin_amdgcn_raw_buffer_load_b64(rs, WS_SG + (((r16 >> 3) * 2 + (i & 1)) * FF + ((i >> 1) ? frow1 : frow0)) * 8, 0, AUX_SC1);
    const unsigned short* p = wo + (size_t)(64 * rb + 8 * w + gq) * DM + 256 * cb + 8 * r16;
#pragma unroll
    for (int i = 0; i < 4; ++i) w2r[i] = ldnt(w2 + (size_t)((i >> 1) ? frow1 : frow0) * DM + 256 * cb + (i & 1) * 128 + 8 * r16);
#pragma unroll
    for (int i = 0; i < 4; ++i) wor[i] = ldnt(p + (size_t)(i >> 1) * 4 * DM + (i & 1) * 128);
    SB();
  }
  PV(3); SB();
  {
    l_run = x16_32_sum(l_run);
    float* orw = reinterpret_cast<float*>(lds + L_OR) + w * OR_W + gq * 272 + r16 * 16;
#pragma unroll
    for (int a = 0; a < 4; ++a) *reinterpret_cast<f32x4*>(orw + 4 * a) = oacc[a];
    if (lane < 8) *reinterpret_cast<float2*>(lds + L_ML + (lane * 8 + w) * 8) = make_float2(m_run, l_run);
  }
  BAR();
  {
    f32x4 ml[4];
#pragma unroll
    for (int i = 0; i < 4; ++i) ml[i] = *reinterpret_cast<const f32x4*>(lds + L_ML + w * 64 + i * 16);
    const float* orr = reinterpret_cast<const float*>(lds + L_OR) + (lane >> 4) * 272 + w * 16 + (lane & 3) * 4 + ((lane >> 2) & 3);
    float oa[8], ob[8];
#pragma unroll
    for (int ww = 0; ww < 8; ++ww) { oa[ww] = orr[ww * OR_W]; ob[ww] = orr[ww * OR_W + 128]; }
    float M = fmaxf(fmaxf(fmaxf(ml[0][0], ml[0][2]), fmaxf(ml[1][0], ml[1][2])), fmaxf(fmaxf(ml[2][0], ml[2][2]), fmaxf(ml[3][0], ml[3][2])));
    float L = 0.f, o0 = 0.f, o1 = 0.f;
#pragma unroll
    for (int ww = 0; ww < 8; ++ww) {
      const float e = __builtin_amdgcn_exp2f((ml[ww >> 1][(ww & 1) * 2] - M) * LOG2E);
      L = fmaf(e, ml[ww >> 1][(ww & 1) * 2 + 1], L);
      o0 = fmaf(e, oa[ww], o0);
      o1 = fmaf(e, ob[ww], o1);
    }
    const unsigned slot = (unsigned)((ab * 32 + kvh * 8 + w) * 32 + split);
    v2u gr = {cvt_pk(o0, o1), tag};
    __builtin_amdgcn_raw_buffer_store_b64(gr, rs, WS_PO + slot * 512 + lane * 8, 0, AUX_SC1);
    if (lane == 0) {
      v4u g2 = {__float_as_uint(M), tag, __float_as_uint(L), tag};
      __builtin_amdgcn_raw_buffer_store_b128(g2, rs, WS_PML + slot * 16, 0, AUX_SC1);
    }
  }
  f32x4 acc[2] = {f32x4{0.f, 0.f, 0.f, 0.f}, f32x4{0.f, 0.f, 0.f, 0.f}};
  {
    constexpr unsigned OOR = 0x7ffffff0u;
    const unsigned slot0 = ((w >> 2) * 32 + h) * 32 + 8 * (w & 3);
    const unsigned pml = lane < 8 ? WS_PML + (slot0 + lane) * 16 : OOR;
    const unsigned po = WS_PO + (slot0 + gq) * 512 + dh * 256 + r16 * 16;
    v4u ml = {0u, ~tag, 0u, ~tag}, og0 = ml, og1 = ml;
    v4u p_m, p_0, p_1;
    p_m = __builtin_amdgcn_raw_buffer_load_b128(rs, pml, 0, AUX_SC1);
    const float xres = bf_lo(x[(g & 1) * DM + ((g >> 1) & 63) * 16 + 2 * (lane & 7) + (lane >> 5)]);
    SB();
    unsigned mk[4];
#pragma unroll
    for (int r = 0; r < 4; ++r) mk[r] = (r == ((r16 & 7) >> 1)) ? ((r16 & 1) ? 0xffff0000u : 0x0000ffffu) : 0u;
#define VALID(v) (((v)[1] == tag) & ((v)[3] == tag))
#define REQUEST() { \
      const unsigned lm_ = __popc(rdy_) >= 6 ? 0xffu : rdy_; \
      const unsigned a0_ = (!VALID(og0) & (bool)((lm_ >> gq) & 1u)) ? po : OOR; \
      const unsigned a1_ = (!VALID(og1) & (bool)((lm_ >> (gq + 4)) & 1u)) ? po + 4 * 512 : OOR; \
      p_m = __builtin_amdgcn_raw_buffer_load_b128(rs, VALID(ml) ? OOR : pml, 0, AUX_SC1); \
      p_0 = __builtin_amdgcn_raw_buffer_load_b128(rs, a0_, 0, AUX_SC1); \
      p_1 = __builtin_amdgcn_raw_buffer_load_b128(rs, a1_, 0, AUX_SC1); }
    {
      if (VALID(p_m)) ml = p_m;
      const unsigned rdy_ = (unsigned)__ballot(VALID(ml)) & 0xffu;
      REQUEST()
    }
    SB();
    if (!__all(hg[0][1] == tag && hg[1][1] == tag && hg[2][1] == tag && hg[3][1] == tag)) {
      for (int spin = 0; spin < SPIN_CAP; ++spin) {
        asm volatile("" ::: "memory");
#pragma unroll
        for (int i = 0; i < 4; ++i)
          hg[i] = __builtin_amdgcn_raw_buffer_load_b64(rs, WS_SG + (((r16 >> 3) * 2 + (i & 1)) * FF + ((i >> 1) ? frow1 : frow0)) * 8, 0, AUX_SC1);
        if (__all(hg[0][1] == tag && hg[1][1] == tag && hg[2][1] == tag && hg[3][1] == tag)) break;
        __builtin_amdgcn_s_sleep(1);
      }
    }
#pragma unroll
    for (int rg = 0; rg < 2; ++rg) {
      const float hv = rbf(__uint_as_float(hg[2 * rg][0]) + __uint_as_float(hg[2 * rg + 1][0]));
      const float sv = hv / (1.f + __expf(-hv));
      unsigned pkv = cvt_pk(sv, sv);
      if (rg == 1 && gq == 3) pkv = 0u;
      const v4u bf = {pkv & mk[0], pkv & mk[1], pkv & mk[2], pkv & mk[3]};
      acc[0] = mfma32(w2r[2 * rg], bf, acc[0]);
      acc[1] = mfma32(w2r[2 * rg + 1], bf, acc[1]);
    }
    SB();
    for (int spin = 0; spin < SPIN_CAP; ++spin) {
      if (VALID(p_m)) ml = p_m;
      if (VALID(p_0)) og0 = p_0;
      if (VALID(p_1)) og1 = p_1;
      const unsigned rdy_ = (unsigned)__ballot(VALID(ml)) & 0xffu;
      if (rdy_ == 0xffu && __all(VALID(og0) & VALID(og1))) break;
      asm volatile("" ::: "memory");
      REQUEST()
    }
    {
      const float ms = __uint_as_float(ml[0]), ls = __uint_as_float(ml[2]);
      float M = fmaxf(ms, dpp<XOR1>(ms)); M = fmaxf(M, dpp<XOR2>(M)); M = fmaxf(M, dpp<HMIRROR>(M));
      const float e = __builtin_amdgcn_exp2f((ms - M) * LOG2E);
      float L = e * ls; L += dpp<XOR1>(L); L += dpp<XOR2>(L); L += dpp<HMIRROR>(L);
      const float wa = __int_as_float(__builtin_amdgcn_ds_bpermute(gq * 4, __float_as_int(e)));
      const float wb = __int_as_float(__builtin_amdgcn_ds_bpermute((gq + 4) * 4, __float_as_int(e)));
      const float v0 = fmaf(wa, bf_lo(og0[0]), wb * bf_lo(og1[0])), v1 = fmaf(wa, bf_hi(og0[0]), wb * bf_hi(og1[0]));
      const float v2 = fmaf(wa, bf_lo(og0[2]), wb * bf_lo(og1[2])), v3 = fmaf(wa, bf_hi(og0[2]), wb * bf_hi(og1[2]));
      const float t0 = swap32_add(v0, v2), t1 = swap32_add(v1, v3);
      const float o = swap16_add(t0, t1);
      reinterpret_cast<float*>(lds + L_OC)[((w >> 2) * 64 + 4 * r16 + 2 * (lane >> 5) + ((lane >> 4) & 1)) * 4 + (w & 3)] = o;
      if (lane == 0) *reinterpret_cast<float2*>(lds + L_WG + w * 8) = make_float2(M, L);
    }
    BAR();
    {
      const f32x4 a = *reinterpret_cast<const f32x4*>(lds + L_WG + (r16 >> 3) * 32), b = *reinterpret_cast<const f32x4*>(lds + L_WG + (r16 >> 3) * 32 + 16);
      const float M = fmaxf(fmaxf(a[0], a[2]), fmaxf(b[0], b[2]));
      float s0 = __builtin_amdgcn_exp2f((a[0] - M) * LOG2E), s1 = __builtin_amdgcn_exp2f((a[2] - M) * LOG2E);
      float s2 = __builtin_amdgcn_exp2f((b[0] - M) * LOG2E), s3 = __builtin_amdgcn_exp2f((b[2] - M) * LOG2E);
      const float inv = __builtin_amdgcn_rcpf(fmaf(s0, a[1], fmaf(s1, a[3], fmaf(s2, b[1], s3 * b[3]))));
      s0 *= inv; s1 *= inv; s2 *= inv; s3 *= inv;
#pragma unroll
      for (int rg = 0; rg < 2; ++rg) {
        const f32x4 o4 = *reinterpret_cast<const f32x4*>(lds + L_OC + ((r16 >> 3) * 64 + 8 * w + 4 * rg + gq) * 16);
        const float o = fmaf(s0, o4[0], fmaf(s1, o4[1], fmaf(s2, o4[2], s3 * o4[3])));
        const unsigned pkv = cvt_pk(o, o);
        const v4u bf = {pkv & mk[0], pkv & mk[1], pkv & mk[2], pkv & mk[3]};
        acc[0] = mfma32(wor[2 * rg], bf, acc[0]);
        acc[1] = mfma32(wor[2 * rg + 1], bf, acc[1]);
      }
    }
    *reinterpret_cast<f32x4*>(lds + L_RED + (w * 128 + lane) * 16) = acc[0];
    *reinterpret_cast<f32x4*>(lds + L_RED + (w * 128 + 64 + lane) * 16) = acc[1];
    BAR();
    if (tid < 256) {
      const int b = tid >> 7, n = (tid & 127) * 2, c = (n & 127) >> 3;
      const float* red = reinterpret_cast<const float*>(lds + L_RED) + ((n >> 7) * 64 + (c >> 2) * 16 + b * 8 + (n & 7)) * 4 + (c & 3);
      float v0 = 0.f, v1 = 0.f;
#pragma unroll
      for (int ww = 0; ww < 8; ++ww) { v0 += red[ww * 512]; v1 += red[ww * 512 + 4]; }
      v4u gr = {__float_as_uint(v0), tag, __float_as_uint(v1), tag};
      __builtin_amdgcn_raw_buffer_store_b128(gr, rs, WS_P2 + ((rb * 2 + b) * 1024 + 256 * cb + n) * 8, 0, AUX_SC1);
    }
    if (w == 0 && g < 128) {
      const int b = g & 1, c0 = (g >> 1) * 16;
      const unsigned off = WS_P2 + (((lane >> 3) * 2 + b) * 1024 + c0) * 8 + (lane & 7) * 16;
      v4u pg[8];
#pragma unroll
      for (int i = 0; i < 8; ++i) pg[i] = __builtin_amdgcn_raw_buffer_load_b128(rs, off + i * (8 * 2 * 1024 * 8), 0, AUX_SC1);
      for (int spin = 0; spin < SPIN_CAP; ++spin) {
        bool ok = true;
#pragma unroll
        for (int i = 0; i < 8; ++i) ok &= VALID(pg[i]);
        if (__all(ok)) break;
        asm volatile("" ::: "memory");
#pragma unroll
        for (int i = 0; i < 8; ++i) {
          const bool vi = VALID(pg[i]);
          const v4u nw = __builtin_amdgcn_raw_buffer_load_b128(rs, vi ? OOR : off + i * (8 * 2 * 1024 * 8), 0, AUX_SC1);
          if (!vi) pg[i] = nw;
        }
      }
      float v0 = 0.f, v1 = 0.f;
#pragma unroll
      for (int i = 0; i < 8; ++i) { v0 += __uint_as_float(pg[i][0]); v1 += __uint_as_float(pg[i][2]); }
      float r = swap32_add(v0, v1);
      r = swap16_add(r, r);
      r += dpp<ROR8>(r);
      if ((lane & 24) == 0) out[b * DM + c0 + 2 * (lane & 7) + (lane >> 5)] = (unsigned short)(cvt_pk(xres + r, 0.f) & 0xffffu);
      if (g == 0 && lane == 0) __hip_atomic_store((unsigned*)(ws + WS_EPOCH), tag, __ATOMIC_RELAXED, __HIP_MEMORY_SCOPE_AGENT);
    }
  }
}

}

extern "C" void kernel_launch(void* const* d_in, const int* in_sizes, int n_in, void* d_out, int out_size, void* d_ws, size_t ws_size,
                              hipStream_t stream) {
  (void)in_sizes; (void)n_in; (void)out_size; (void)ws_size;
  hipLaunchKernelGGL(fused_decode, dim3(NWG), dim3(NTHR), 0, stream, (const unsigned short*)d_in[0], (const unsigned short*)d_in[1],
                     (const unsigned short*)d_in[2], (const unsigned short*)d_in[3], (const unsigned short*)d_in[4],
                     (const unsigned short*)d_in[6], (const unsigned short*)d_in[7], (unsigned short*)d_out, (unsigned char*)d_ws);
}
```

```cpp
#include <hip/hip_runtime.h>
#include <hip/hip_bf16.h>
#include <stdint.h>

namespace {

typedef unsigned v4u __attribute__((ext_vector_type(4)));
typedef unsigned v2u __attribute__((ext_vector_type(2)));
typedef float f32x4 __attribute__((ext_vector_type(4)));
typedef short s16x4 __attribute__((ext_vector_type(4)));
typedef __bf16 bf16x8 __attribute__((ext_vector_type(8)));
typedef __bf16 bf16x2 __attribute__((ext_vector_type(2)));

constexpr int DM = 1024;
constexpr int NQ = 4096;
constexpr int SK = 32768;
constexpr int FF = 3584;
constexpr int NWG = 256, NTHR = 512;

constexpr unsigned WS_EPOCH = 0;
constexpr unsigned WS_QG = 4096;
constexpr unsigned WS_SG = WS_QG + 131072;
constexpr unsigned WS_PML = WS_SG + 131072;
constexpr unsigned WS_PO = WS_PML + 32768;
constexpr unsigned WS_P2 = WS_PO + 1024u * 1024;
constexpr unsigned WS_END = WS_P2 + 48u * 2 * 1024 * 8;

constexpr int L_RQ = 0;
constexpr int L_Q = 4096;
constexpr int L_ML = 6144;
constexpr int L_WG = 6656;
constexpr int L_OC = 7168;
constexpr int L_RED = 11264;
constexpr int L_OR = L_RED + 16384;
constexpr int OR_W = 4 * 272;
constexpr int L_BYTES = L_OR + 8 * OR_W * 4;

constexpr int AUX_SC1 = 16, AUX_NT = 2;
constexpr int SPIN_CAP = 1 << 16;
#define LATE_THR 56
constexpr float LOG2E = 1.4426950408889634f;
constexpr float QK_SCALE = 0.08838834764831845f;

__device__ __forceinline__ float bf_lo(unsigned u) { return __uint_as_float(u << 16); }
__device__ __forceinline__ float bf_hi(unsigned u) { return __uint_as_float(u & 0xffff0000u); }
__device__ __forceinline__ unsigned cvt_pk(float lo, float hi) {
  bf16x2 p = {(__bf16)lo, (__bf16)hi};
  return __builtin_bit_cast(unsigned, p);
}
__device__ __forceinline__ float rbf(float x) { return bf_lo(cvt_pk(x, x)); }

template <int CTRL> __device__ __forceinline__ float dpp(float x) {
  return __builtin_bit_cast(float, __builtin_amdgcn_mov_dpp(__builtin_bit_cast(int, x), CTRL, 0xf, 0xf, true));
}
constexpr int XOR1 = 0xB1, XOR2 = 0x4E, HMIRROR = 0x141, MIRROR = 0x140, ROR8 = 0x128;
__device__ __forceinline__ float swap16_add(float a, float b) {
  auto s = __builtin_amdgcn_permlane16_swap(__float_as_uint(a), __float_as_uint(b), false, false);
  return __uint_as_float(s[0]) + __uint_as_float(s[1]);
}
__device__ __forceinline__ float swap32_add(float a, float b) {
  auto s = __builtin_amdgcn_permlane32_swap(__float_as_uint(a), __float_as_uint(b), false, false);
  return __uint_as_float(s[0]) + __uint_as_float(s[1]);
}
__device__ __forceinline__ float swap16_max(float a) {
  auto s = __builtin_amdgcn_permlane16_swap(__float_as_uint(a), __float_as_uint(a), false, false);
  return fmaxf(__uint_as_float(s[0]), __uint_as_float(s[1]));
}
__device__ __forceinline__ float swap32_max(float a) {
  auto s = __builtin_amdgcn_permlane32_swap(__float_as_uint(a), __float_as_uint(a), false, false);
  return fmaxf(__uint_as_float(s[0]), __uint_as_float(s[1]));
}
__device__ __forceinline__ float x16_32_sum(float x) { x = swap16_add(x, x); return swap32_add(x, x); }
__device__ __forceinline__ float x16_32_max(float x) { return swap32_max(swap16_max(x)); }
__device__ __forceinline__ float row16_sum(float x) {
  x += dpp<XOR1>(x); x += dpp<XOR2>(x); x += dpp<HMIRROR>(x); x += dpp<MIRROR>(x);
  return x;
}
__device__ __forceinline__ float row16_max(float x) {
  x = fmaxf(x, dpp<XOR1>(x)); x = fmaxf(x, dpp<XOR2>(x)); x = fmaxf(x, dpp<HMIRROR>(x)); x = fmaxf(x, dpp<MIRROR>(x));
  return x;
}
__device__ __forceinline__ float wave_sum(float x) { return x16_32_sum(row16_sum(x)); }

__device__ __forceinline__ v4u ldnt(const void* p) { return __builtin_nontemporal_load((const v4u*)p); }
__device__ __forceinline__ f32x4 mfma32(v4u a, v4u b, f32x4 c) {
  return __builtin_amdgcn_mfma_f32_16x16x32_bf16(__builtin_bit_cast(bf16x8, a), __builtin_bit_cast(bf16x8, b), c, 0, 0, 0);
}
__device__ __forceinline__ f32x4 mfma16(v2u a, v2u b, f32x4 c) {
  return __builtin_amdgcn_mfma_f32_16x16x16bf16_1k(__builtin_bit_cast(s16x4, a), __builtin_bit_cast(s16x4, b), c, 0, 0, 0);
}
#define SB() __builtin_amdgcn_sched_barrier(0)
#define STAMP(i) do { } while (0)
#define BAR() asm volatile("s_waitcnt lgkmcnt(0)\n\ts_barrier" ::: "memory")

__device__ __forceinline__ void slice_reduce(const float (&acc)[16], float* red, int w, int lane) {
  float a8[8], a4[4];
#pragma unroll
  for (int j = 0; j < 8; ++j) a8[j] = swap32_add(acc[j], acc[8 + j]);
#pragma unroll
  for (int j = 0; j < 4; ++j) a4[j] = swap16_add(a8[j], a8[4 + j]);
#pragma unroll
  for (int j = 0; j < 4; ++j) a4[j] += dpp<ROR8>(a4[j]);
  if ((lane & 8) == 0) {
    const int b = lane >> 5, r1 = (lane >> 4) & 1, piece = lane & 7;
    *reinterpret_cast<f32x4*>(red + w * 128 + b * 64 + piece * 8 + r1 * 4) = f32x4{a4[0], a4[1], a4[2], a4[3]};
  }
}

__global__ void __launch_bounds__(NTHR, 2)
fused_decode(const unsigned short* __restrict__ x, const unsigned short* __restrict__ kc, const unsigned short* __restrict__ vc,
             const unsigned short* __restrict__ wq, const unsigned short* __restrict__ wo, const unsigned short* __restrict__ w1,
             const unsigned short* __restrict__ w2, unsigned short* __restrict__ out, unsigned char* ws) {
  __shared__ __attribute__((aligned(16))) unsigned char lds[L_BYTES];
  const int tid = threadIdx.x, lane = tid & 63, w = __builtin_amdgcn_readfirstlane(tid >> 6);
  const int g = blockIdx.x, xc = g & 7, gi = g >> 3;
  const __amdgpu_buffer_rsrc_t rs = __builtin_amdgcn_make_buffer_rsrc(ws, 0, WS_END, 0x00020000);

  STAMP(0);
  const int ab = xc >> 2, kvh = xc & 3, split = gi;
  const size_t kvbase = ((size_t)(ab * 4 + kvh) * SK + (size_t)split * 1024 + 32 * w) * 128;
  const int r16 = lane & 15, gq = lane >> 4;
  const unsigned short* kp = kc + kvbase + (4 * (r16 & 3) + (r16 >> 2)) * 128 + gq * 8;
  const unsigned short* vp = vc + kvbase + lane * 8;
  v4u kr[4][8], vr[4][8];
#define LD_K(c, i_) kr[c][i_] = ldnt(kp + (c) * 256 * 128 + ((i_) >> 2) * 16 * 128 + ((i_) & 3) * 32)
#define ISSUE_K(c) do { _Pragma("unroll") for (int i_ = 0; i_ < 8; ++i_) LD_K(c, i_); SB(); } while (0)
#define ISSUE_V(c) do { _Pragma("unroll") for (int i_ = 0; i_ < 8; ++i_) vr[c][i_] = ldnt(vp + (c) * 256 * 128 + i_ * 512); SB(); } while (0)

  const unsigned epoch = __hip_atomic_load((const unsigned*)(ws + WS_EPOCH), __ATOMIC_RELAXED, __HIP_MEMORY_SCOPE_AGENT);
  unsigned tag;
  {
    const bool is_q = g < 128, has_a = g < 240;
    const int u = is_q ? g : g - 128;
    const int ar = u & 1, ac = u >> 1;
    const int ld = is_q ? NQ : FF;
    const __amdgpu_buffer_rsrc_t rw = __builtin_amdgcn_make_buffer_rsrc((void*)(is_q ? wq : w1), 0, has_a ? DM * ld * 2 : 0, 0x00020000);
    v4u xs[4];
#pragma unroll
    for (int i = 0; i < 4; ++i) xs[i] = *reinterpret_cast<const v4u*>(x + (lane + 64 * i) * 8);
    const int xrow = 512 * ar + 64 * w + lane;
    const unsigned short xr0 = x[xrow], xr1 = x[DM + xrow];
    SB();
    v4u wr[8];
    {
      int off = ((512 * ar + 64 * w + (lane >> 3)) * ld + 64 * ac + 8 * (lane & 7)) * 2;
#pragma unroll
      for (int i = 0; i < 8; ++i) { wr[i] = __builtin_amdgcn_raw_buffer_load_b128(rw, off, 0, AUX_NT); off += 16 * ld; }
    }
    SB();
    float s0 = 0.f, s1 = 0.f;
#pragma unroll
    for (int i = 0; i < 2; ++i)
#pragma unroll
      for (int k = 0; k < 4; ++k) { s0 += bf_lo(xs[i][k]) + bf_hi(xs[i][k]); s1 += bf_lo(xs[2 + i][k]) + bf_hi(xs[2 + i][k]); }
    const float mean0 = wave_sum(s0) * (1.f / DM), mean1 = wave_sum(s1) * (1.f / DM);
    float v0 = 0.f, v1 = 0.f;
#pragma unroll
    for (int i = 0; i < 2; ++i)
#pragma unroll
      for (int k = 0; k < 4; ++k) {
        float d;
        d = bf_lo(xs[i][k]) - mean0; v0 = fmaf(d, d, v0);
        d = bf_hi(xs[i][k]) - mean0; v0 = fmaf(d, d, v0);
        d = bf_lo(xs[2 + i][k]) - mean1; v1 = fmaf(d, d, v1);
        d = bf_hi(xs[2 + i][k]) - mean1; v1 = fmaf(d, d, v1);
      }
    const float var0 = rbf(wave_sum(v0) * (1.f / DM)), var1 = rbf(wave_sum(v1) * (1.f / DM));
    const float m0 = rbf(mean0), m1 = rbf(mean1);
    const float sd0 = rbf(sqrtf(rbf(var0 + 1e-5f))), sd1 = rbf(sqrtf(rbf(var1 + 1e-5f)));
    const unsigned xnp = cvt_pk(rbf(bf_lo(xr0) - m0) / sd0, rbf(bf_lo(xr1) - m1) / sd1);
    unsigned xb[8];
#pragma unroll
    for (int i = 0; i < 8; ++i) xb[i] = __builtin_amdgcn_ds_bpermute((8 * i + (lane >> 3)) * 4, xnp);
    tag = __builtin_amdgcn_readfirstlane(epoch) * 1664525u + 1013904223u;
    SB();
    float acc[16];
#pragma unroll
    for (int j = 0; j < 16; ++j) acc[j] = 0.f;
#pragma unroll
    for (int i = 0; i < 8; ++i) {
      const float x0 = bf_lo(xb[i]), x1 = bf_hi(xb[i]);
#pragma unroll
      for (int k = 0; k < 4; ++k) {
        const float lo = bf_lo(wr[i][k]), hi = bf_hi(wr[i][k]);
        acc[2 * k] = fmaf(x0, lo, acc[2 * k]);
        acc[2 * k + 1] = fmaf(x0, hi, acc[2 * k + 1]);
        acc[8 + 2 * k] = fmaf(x1, lo, acc[8 + 2 * k]);
        acc[8 + 2 * k + 1] = fmaf(x1, hi, acc[8 + 2 * k + 1]);
      }
      SB();
      LD_K(0, i);
      SB();
    }
    float* rq = reinterpret_cast<float*>(lds + L_RQ);
    slice_reduce(acc, rq, w, lane);
    BAR();
    if (w == 0 && has_a) {
      float p0 = 0.f, p1 = 0.f;
#pragma unroll
      for (int ww = 0; ww < 8; ++ww) {
        const float2 v = *reinterpret_cast<const float2*>(rq + ww * 128 + 2 * lane);
        p0 += v.x; p1 += v.y;
      }
      const unsigned off = (is_q ? WS_QG : WS_SG) + (((lane >> 5) * 2 + ar) * ld + 64 * ac + 2 * (lane & 31)) * 8;
      v4u gr = {__float_as_uint(p0), tag, __float_as_uint(p1), tag};
      __builtin_amdgcn_raw_buffer_store_b128(gr, rs, off, 0, AUX_SC1);
    }
    SB();
  }
  STAMP(1);
  ISSUE_V(0);
  ISSUE_K(1);
  STAMP(2);

  const unsigned qoff = WS_QG + (ab * 2 * NQ + kvh * 1024 + 2 * tid) * 8;
  v4u qg[2];
#pragma unroll
  for (int r = 0; r < 2; ++r) qg[r] = __builtin_amdgcn_raw_buffer_load_b128(rs, qoff + r * NQ * 8, 0, AUX_SC1);
  SB();
  asm volatile("s_barrier" ::: "memory");
  SB();
  ISSUE_V(1);
  STAMP(3);
#define QG_OK() (qg[0][1] == tag && qg[0][3] == tag && qg[1][1] == tag && qg[1][3] == tag)
  if (!__all(QG_OK())) {
    for (int spin = 0; spin < SPIN_CAP; ++spin) {
      asm volatile("" ::: "memory");
#pragma unroll
      for (int r = 0; r < 2; ++r) qg[r] = __builtin_amdgcn_raw_buffer_load_b128(rs, qoff + r * NQ * 8, 0, AUX_SC1);
      if (__all(QG_OK())) break;
      __builtin_amdgcn_s_sleep(2);
    }
  }
  reinterpret_cast<unsigned*>(lds + L_Q)[tid] =
      cvt_pk(__uint_as_float(qg[0][0]) + __uint_as_float(qg[1][0]), __uint_as_float(qg[0][2]) + __uint_as_float(qg[1][2]));
  BAR();
  STAMP(4);
  v4u qf[4];
#pragma unroll
  for (int s = 0; s < 4; ++s) qf[s] = *reinterpret_cast<const v4u*>(lds + L_Q + (r16 & 7) * 256 + s * 64 + gq * 16);

  float m_run = -INFINITY, l_run = 0.f;
  f32x4 oacc[4];
#pragma unroll
  for (int a = 0; a < 4; ++a) oacc[a] = f32x4{0.f, 0.f, 0.f, 0.f};
  unsigned pk[8];
  const unsigned pmask = (lane & 8) ? 0xffff0000u : 0x0000ffffu;

#define QK(c) do { \
    f32x4 sc_[2]; \
    _Pragma("unroll") for (int t_ = 0; t_ < 2; ++t_) { \
      sc_[t_] = f32x4{0.f, 0.f, 0.f, 0.f}; \
      _Pragma("unroll") for (int s_ = 0; s_ < 4; ++s_) sc_[t_] = mfma32(kr[c][4 * t_ + s_], qf[s_], sc_[t_]); \
    } \
    float sv_[8]; float mx_ = -INFINITY; \
    _Pragma("unroll") for (int i_ = 0; i_ < 8; ++i_) { sv_[i_] = rbf(rbf(sc_[i_ >> 2][i_ & 3]) * QK_SCALE); mx_ = fmaxf(mx_, sv_[i_]); } \
    mx_ = x16_32_max(mx_); \
    const float mn_ = fmaxf(m_run, mx_); \
    const float al_ = __builtin_amdgcn_exp2f((m_run - mn_) * LOG2E); \
    m_run = mn_; \
    float ps_ = 0.f; \
    _Pragma("unroll") for (int i_ = 0; i_ < 8; ++i_) { \
      const float p_ = __builtin_amdgcn_exp2f((sv_[i_] - mn_) * LOG2E); ps_ += p_; pk[i_] = cvt_pk(p_, p_) & pmask; } \
    l_run = fmaf(l_run, al_, ps_); \
    _Pragma("unroll") for (int a_ = 0; a_ < 4; ++a_) oacc[a_] *= al_; \
  } while (0)
#define PV(c) do { \
    _Pragma("unroll") for (int i_ = 0; i_ < 8; ++i_) { \
      const v2u lo_ = {vr[c][i_][0], vr[c][i_][1]}, hi_ = {vr[c][i_][2], vr[c][i_][3]}; \
      const v2u b0_ = {pk[i_], 0u}, b1_ = {0u, pk[i_]}; \
      oacc[0] = mfma16(lo_, b0_, oacc[0]); oacc[1] = mfma16(lo_, b1_, oacc[1]); \
      oacc[2] = mfma16(hi_, b0_, oacc[2]); oacc[3] = mfma16(hi_, b1_, oacc[3]); \
    } \
  } while (0)

  const bool isP = gi & 1;
  const int idx = (gi >> 1) * 8 + xc;
  constexpr unsigned OOR = 0x7ffffff0u;
  v4u tw[8];

  QK(0); SB(); ISSUE_K(2);
  PV(0); SB(); ISSUE_V(2);
  QK(1); SB(); ISSUE_K(3);
  PV(1); SB(); ISSUE_V(3);
  QK(2); SB();
  PV(2); SB();
  QK(3); SB();
  asm volatile("s_barrier" ::: "memory");
  SB();
  {
    const __amdgpu_buffer_rsrc_t rwo = __builtin_amdgcn_make_buffer_rsrc((void*)wo, 0, isP ? NQ * DM * 2 : 0, 0x00020000);
    const int off = ((128 * (idx >> 2) + 16 * w + gq) * DM + 256 * (idx & 3) + 8 * r16) * 2;
#pragma unroll
    for (int i = 0; i < 8; ++i) tw[i] = __builtin_amdgcn_raw_buffer_load_b128(rwo, off + (i >> 1) * 4 * DM * 2 + (i & 1) * 256, 0, AUX_NT);
    SB();
  }
  PV(3); SB();

  STAMP(5);
  {
    l_run = x16_32_sum(l_run);
    float* orw = reinterpret_cast<float*>(lds + L_OR) + w * OR_W + gq * 272 + r16 * 16;
#pragma unroll
    for (int a = 0; a < 4; ++a) *reinterpret_cast<f32x4*>(orw + 4 * a) = oacc[a];
    if (lane < 8) *reinterpret_cast<float2*>(lds + L_ML + (lane * 8 + w) * 8) = make_float2(m_run, l_run);
  }
  BAR();
  {
    f32x4 ml[4];
#pragma unroll
    for (int i = 0; i < 4; ++i) ml[i] = *reinterpret_cast<const f32x4*>(lds + L_ML + w * 64 + i * 16);
    const float* orr = reinterpret_cast<const float*>(lds + L_OR) + (lane >> 4) * 272 + w * 16 + (lane & 3) * 4 + ((lane >> 2) & 3);
    float oa[8], ob[8];
#pragma unroll
    for (int ww = 0; ww < 8; ++ww) { oa[ww] = orr[ww * OR_W]; ob[ww] = orr[ww * OR_W + 128]; }
    float M = fmaxf(fmaxf(fmaxf(ml[0][0], ml[0][2]), fmaxf(ml[1][0], ml[1][2])), fmaxf(fmaxf(ml[2][0], ml[2][2]), fmaxf(ml[3][0], ml[3][2])));
    float L = 0.f, o0 = 0.f, o1 = 0.f;
#pragma unroll
    for (int ww = 0; ww < 8; ++ww) {
      const float e = __builtin_amdgcn_exp2f((ml[ww >> 1][(ww & 1) * 2] - M) * LOG2E);
      L = fmaf(e, ml[ww >> 1][(ww & 1) * 2 + 1], L);
      o0 = fmaf(e, oa[ww], o0);
      o1 = fmaf(e, ob[ww], o1);
    }
    const unsigned slot = (unsigned)((ab * 32 + kvh * 8 + w) * 32 + split);
    v2u gr = {cvt_pk(o0, o1), tag};
    __builtin_amdgcn_raw_buffer_store_b64(gr, rs, WS_PO + slot * 512 + lane * 8, 0, AUX_SC1);
    if (lane == 0) {
      v4u g2 = {__float_as_uint(M), tag, __float_as_uint(L), tag};
      __builtin_amdgcn_raw_buffer_store_b128(g2, rs, WS_PML + slot * 16, 0, AUX_SC1);
    }
  }

  STAMP(6);
  unsigned mk[4];
#pragma unroll
  for (int r = 0; r < 4; ++r) mk[r] = (r == ((r16 & 7) >> 1)) ? ((r16 & 1) ? 0xffff0000u : 0x0000ffffu) : 0u;
#define VALID(v) (((v)[1] == tag) & ((v)[3] == tag))
  if (isP) {
    const int h = idx >> 2, cb = idx & 3;
    const unsigned slot0 = ((w >> 2) * 32 + h) * 32 + 8 * (w & 3);
    const unsigned pml = lane < 8 ? WS_PML + (slot0 + lane) * 16 : OOR;
    const unsigned po = WS_PO + (slot0 + (lane >> 5)) * 512 + (lane & 31) * 16;
    v4u ml = {0u, ~tag, 0u, ~tag}, og[4] = {ml, ml, ml, ml};
    v4u p_m, p_o[4] = {ml, ml, ml, ml};
    p_m = __builtin_amdgcn_raw_buffer_load_b128(rs, pml, 0, AUX_SC1);
    for (int spin = 0; spin < SPIN_CAP; ++spin) {
      if (VALID(p_m)) ml = p_m;
      bool all = true;
#pragma unroll
      for (int i = 0; i < 4; ++i) { if (VALID(p_o[i])) og[i] = p_o[i]; all &= VALID(og[i]); }
      const unsigned rdy = (unsigned)__ballot(VALID(ml)) & 0xffu;
      if (rdy == 0xffu && __all(all)) break;
      asm volatile("" ::: "memory");
      const unsigned lm = (__popc(rdy) >= 6 ? 0xffu : rdy) >> (lane >> 5);
      p_m = __builtin_amdgcn_raw_buffer_load_b128(rs, VALID(ml) ? OOR : pml, 0, AUX_SC1);
#pragma unroll
      for (int i = 0; i < 4; ++i)
        p_o[i] = __builtin_amdgcn_raw_buffer_load_b128(rs, (!VALID(og[i]) & (bool)((lm >> (2 * i)) & 1u)) ? po + i * 1024 : OOR, 0, AUX_SC1);
    }
    STAMP(7);
    {
      const float ms = __uint_as_float(ml[0]), ls = __uint_as_float(ml[2]);
      float M = fmaxf(ms, dpp<XOR1>(ms)); M = fmaxf(M, dpp<XOR2>(M)); M = fmaxf(M, dpp<HMIRROR>(M));
      const float e = __builtin_amdgcn_exp2f((ms - M) * LOG2E);
      float L = e * ls; L += dpp<XOR1>(L); L += dpp<XOR2>(L); L += dpp<HMIRROR>(L);
      float v0 = 0.f, v1 = 0.f, v2 = 0.f, v3 = 0.f;
#pragma unroll
      for (int i = 0; i < 4; ++i) {
        const float we = __int_as_float(__builtin_amdgcn_ds_bpermute((2 * i + (lane >> 5)) * 4, __float_as_int(e)));
        v0 = fmaf(we, bf_lo(og[i][0]), v0); v1 = fmaf(we, bf_hi(og[i][0]), v1);
        v2 = fmaf(we, bf_lo(og[i][2]), v2); v3 = fmaf(we, bf_hi(og[i][2]), v3);
      }
      const float t0 = swap32_add(v0, v2), t1 = swap32_add(v1, v3);
      float* oc = reinterpret_cast<float*>(lds + L_OC) + ((w >> 2) * 128 + 4 * (lane & 31) + 2 * (lane >> 5)) * 4 + (w & 3);
      oc[0] = t0; oc[4] = t1;
      if (lane == 0) *reinterpret_cast<float2*>(lds + L_WG + w * 8) = make_float2(M, L);
    }
    BAR();
    f32x4 acc[2] = {f32x4{0.f, 0.f, 0.f, 0.f}, f32x4{0.f, 0.f, 0.f, 0.f}};
    {
      const f32x4 a = *reinterpret_cast<const f32x4*>(lds + L_WG + (r16 >> 3) * 32), b = *reinterpret_cast<const f32x4*>(lds + L_WG + (r16 >> 3) * 32 + 16);
      const float M = fmaxf(fmaxf(a[0], a[2]), fmaxf(b[0], b[2]));
      float s0 = __builtin_amdgcn_exp2f((a[0] - M) * LOG2E), s1 = __builtin_amdgcn_exp2f((a[2] - M) * LOG2E);
      float s2 = __builtin_amdgcn_exp2f((b[0] - M) * LOG2E), s3 = __builtin_amdgcn_exp2f((b[2] - M) * LOG2E);
      const float inv = __builtin_amdgcn_rcpf(fmaf(s0, a[1], fmaf(s1, a[3], fmaf(s2, b[1], s3 * b[3]))));
      s0 *= inv; s1 *= inv; s2 *= inv; s3 *= inv;
#pragma unroll
      for (int j = 0; j < 4; ++j) {
        const f32x4 o4 = *reinterpret_cast<const f32x4*>(lds + L_OC + ((r16 >> 3) * 128 + 16 * w + 4 * j + gq) * 16);
        const float o = fmaf(s0, o4[0], fmaf(s1, o4[1], fmaf(s2, o4[2], s3 * o4[3])));
        const unsigned pkv = cvt_pk(o, o);
        const v4u bf = {pkv & mk[0], pkv & mk[1], pkv & mk[2], pkv & mk[3]};
        acc[0] = mfma32(tw[2 * j], bf, acc[0]);
        acc[1] = mfma32(tw[2 * j + 1], bf, acc[1]);
      }
    }
    *reinterpret_cast<f32x4*>(lds + L_RED + (w * 128 + lane) * 16) = acc[0];
    *reinterpret_cast<f32x4*>(lds + L_RED + (w * 128 + 64 + lane) * 16) = acc[1];
    BAR();
    if (tid < 256) {
      const int b = tid >> 7, n = (tid & 127) * 2, c = (n & 127) >> 3;
      const float* red = reinterpret_cast<const float*>(lds + L_RED) + ((n >> 7) * 64 + (c >> 2) * 16 + b * 8 + (n & 7)) * 4 + (c & 3);
      float v0 = 0.f, v1 = 0.f;
#pragma unroll
      for (int ww = 0; ww < 8; ++ww) { v0 += red[ww * 512]; v1 += red[ww * 512 + 4]; }
      v4u gr = {__float_as_uint(v0), tag, __float_as_uint(v1), tag};
      __builtin_amdgcn_raw_buffer_store_b128(gr, rs, WS_P2 + ((h * 2 + b) * 1024 + 256 * cb + n) * 8, 0, AUX_SC1);
    }
    STAMP(8); STAMP(11); STAMP(9); STAMP(10);
  } else {
    const int rb = idx >> 3, c8 = idx & 7;
    const unsigned hoff = (lane & 31) < 28 ? WS_SG + ((lane >> 5) * FF + 224 * rb + 28 * w + (lane & 31)) * 8 : OOR;
    v2u hg[2];
    hg[0] = __builtin_amdgcn_raw_buffer_load_b64(rs, hoff, 0, AUX_SC1);
    hg[1] = __builtin_amdgcn_raw_buffer_load_b64(rs, hoff, 2 * FF * 8, AUX_SC1);
    v4u w2r[7];
    {
      const unsigned short* p = w2 + (size_t)(224 * rb + 28 * w + gq) * DM + 128 * c8 + 8 * r16;
#pragma unroll
      for (int j = 0; j < 7; ++j) w2r[j] = ldnt(p + (size_t)4 * j * DM);
    }
    const float xres = bf_lo(x[(idx & 1) * DM + (idx >> 1) * 16 + 2 * (lane & 7) + (lane >> 5)]);
    SB();
    const bool hskip = (lane & 31) >= 28;
    if (!__all(hskip | ((hg[0][1] == tag) & (hg[1][1] == tag)))) {
      for (int spin = 0; spin < SPIN_CAP; ++spin) {
        asm volatile("" ::: "memory");
        hg[0] = __builtin_amdgcn_raw_buffer_load_b64(rs, hoff, 0, AUX_SC1);
        hg[1] = __builtin_amdgcn_raw_buffer_load_b64(rs, hoff, 2 * FF * 8, AUX_SC1);
        if (__all(hskip | ((hg[0][1] == tag) & (hg[1][1] == tag)))) break;
        __builtin_amdgcn_s_sleep(1);
      }
    }
    const float h0 = rbf(swap32_add(__uint_as_float(hg[0][0]), __uint_as_float(hg[0][0]))), h1 = rbf(swap32_add(__uint_as_float(hg[1][0]), __uint_as_float(hg[1][0])));
    const int spk = (int)cvt_pk(h0 / (1.f + __expf(-h0)), h1 / (1.f + __expf(-h1)));
    f32x4 acc = {0.f, 0.f, 0.f, 0.f};
#pragma unroll
    for (int j = 0; j < 7; ++j) {
      const unsigned sp = (unsigned)__builtin_amdgcn_ds_bpermute((4 * j + gq) * 4, spk);
      const unsigned v = (r16 & 8) ? (sp >> 16) : (sp & 0xffffu), pkv = v | (v << 16);
      const v4u bf = {pkv & mk[0], pkv & mk[1], pkv & mk[2], pkv & mk[3]};
      acc = mfma32(w2r[j], bf, acc);
    }
    *reinterpret_cast<f32x4*>(lds + L_RED + (w * 64 + lane) * 16) = acc;
    BAR();
    if (tid < 128) {
      const int b = tid >> 6, n = (tid & 63) * 2, c = n >> 3;
      const float* red = reinterpret_cast<const float*>(lds + L_RED) + ((c >> 2) * 16 + b * 8 + (n & 7)) * 4 + (c & 3);
      float v0 = 0.f, v1 = 0.f;
#pragma unroll
      for (int ww = 0; ww < 8; ++ww) { v0 += red[ww * 256]; v1 += red[ww * 256 + 4]; }
      v4u gr = {__float_as_uint(v0), tag, __float_as_uint(v1), tag};
      __builtin_amdgcn_raw_buffer_store_b128(gr, rs, WS_P2 + (((32 + rb) * 2 + b) * 1024 + 128 * c8 + n) * 8, 0, AUX_SC1);
    }
    STAMP(11); STAMP(7); STAMP(8);
    if (w == 0) {
      const int b = idx & 1, c0 = (idx >> 1) * 16;
      const unsigned off = WS_P2 + (((lane >> 3) * 2 + b) * 1024 + c0) * 8 + (lane & 7) * 16;
      v4u pg[6];
      for (int spin = 0; spin < SPIN_CAP; ++spin) {
        asm volatile("" ::: "memory");
        const v4u gt = __builtin_amdgcn_raw_buffer_load_b128(rs, lane < 32 ? WS_P2 + ((lane >> 3) * 16 + b) * 8192 + c0 * 8 + (lane & 7) * 16 : OOR, 0, AUX_SC1);
        if (__any(VALID(gt))) break;
      }
#pragma unroll
      for (int i = 0; i < 6; ++i) pg[i] = __builtin_amdgcn_raw_buffer_load_b128(rs, off + i * (8 * 2 * 1024 * 8), 0, AUX_SC1);
      for (int spin = 0; spin < SPIN_CAP; ++spin) {
        bool ok = true;
#pragma unroll
        for (int i = 0; i < 6; ++i) ok &= VALID(pg[i]);
        if (__all(ok)) break;
        asm volatile("" ::: "memory");
#pragma unroll
        for (int i = 0; i < 6; ++i) {
          const bool vi = VALID(pg[i]);
          const v4u nw = __builtin_amdgcn_raw_buffer_load_b128(rs, vi ? OOR : off + i * (8 * 2 * 1024 * 8), 0, AUX_SC1);
          if (!vi) pg[i] = nw;
        }
      }
      STAMP(9);
      float v0 = 0.f, v1 = 0.f;
#pragma unroll
      for (int i = 0; i < 6; ++i) { v0 += __uint_as_float(pg[i][0]); v1 += __uint_as_float(pg[i][2]); }
      float r = swap32_add(v0, v1);
      r = swap16_add(r, r);
      r += dpp<ROR8>(r);
      if ((lane & 24) == 0) out[b * DM + c0 + 2 * (lane & 7) + (lane >> 5)] = (unsigned short)(cvt_pk(xres + r, 0.f) & 0xffffu);
      STAMP(10);
      if (g == 0 && lane == 0) __hip_atomic_store((unsigned*)(ws + WS_EPOCH), tag, __ATOMIC_RELAXED, __HIP_MEMORY_SCOPE_AGENT);
    }
  }
}

}

extern "C" void kernel_launch(void* const* d_in, const int* in_sizes, int n_in, void* d_out, int out_size, void* d_ws, size_t ws_size,
                              hipStream_t stream) {
  (void)in_sizes; (void)n_in; (void)out_size; (void)ws_size;
  hipLaunchKernelGGL(fused_decode, dim3(NWG), dim3(NTHR), 0, stream, (const unsigned short*)d_in[0], (const unsigned short*)d_in[1],
                     (const unsigned short*)d_in[2], (const unsigned short*)d_in[3], (const unsigned short*)d_in[4],
                     (const unsigned short*)d_in[6], (const unsigned short*)d_in[7], (unsigned short*)d_out, (unsigned char*)d_ws);
}
```

```cpp
#include <hip/hip_runtime.h>
#include <hip/hip_bf16.h>
#include <stdint.h>

namespace {

typedef unsigned v4u __attribute__((ext_vector_type(4)));
typedef unsigned v2u __attribute__((ext_vector_type(2)));
typedef float f32x4 __attribute__((ext_vector_type(4)));
typedef short s16x4 __attribute__((ext_vector_type(4)));
typedef __bf16 bf16x8 __attribute__((ext_vector_type(8)));
typedef __bf16 bf16x2 __attribute__((ext_vector_type(2)));

constexpr int DM = 1024;
constexpr int NQ = 4096;
constexpr int SK = 32768;
constexpr int FF = 3584;
constexpr int NWG = 256, NTHR = 512;

constexpr unsigned WS_EPOCH = 0;
constexpr unsigned WS_QG = 4096;
constexpr unsigned WS_SG = WS_QG + 131072;
constexpr unsigned WS_PML = WS_SG + 131072;
constexpr unsigned WS_PO = WS_PML + 32768;
constexpr unsigned WS_P2 = WS_PO + 1024u * 1024;
constexpr unsigned WS_END = WS_P2 + 48u * 2 * 1024 * 8;

constexpr int L_RQ = 0;
constexpr int L_Q = 4096;
constexpr int L_ML = 6144;
constexpr int L_WG = 6656;
constexpr int L_OC = 7168;
constexpr int L_RED = 11264;
constexpr int L_OR = L_RED + 16384;
constexpr int OR_W = 4 * 272;
constexpr int L_BYTES = L_OR + 8 * OR_W * 4;

constexpr int AUX_SC1 = 16, AUX_NT = 2;
constexpr int SPIN_CAP = 1 << 16;
constexpr float LOG2E = 1.4426950408889634f;
constexpr float QK_SCALE = 0.08838834764831845f;

__device__ __forceinline__ float bf_lo(unsigned u) { return __uint_as_float(u << 16); }
__device__ __forceinline__ float bf_hi(unsigned u) { return __uint_as_float(u & 0xffff0000u); }
__device__ __forceinline__ unsigned cvt_pk(float lo, float hi) {
  bf16x2 p = {(__bf16)lo, (__bf16)hi};
  return __builtin_bit_cast(unsigned, p);
}
__device__ __forceinline__ float rbf(float x) { return bf_lo(cvt_pk(x, x)); }

template <int CTRL> __device__ __forceinline__ float dpp(float x) {
  return __builtin_bit_cast(float, __builtin_amdgcn_mov_dpp(__builtin_bit_cast(int, x), CTRL, 0xf, 0xf, true));
}
constexpr int XOR1 = 0xB1, XOR2 = 0x4E, HMIRROR = 0x141, MIRROR = 0x140, ROR8 = 0x128;
__device__ __forceinline__ float swap16_add(float a, float b) {
  auto s = __builtin_amdgcn_permlane16_swap(__float_as_uint(a), __float_as_uint(b), false, false);
  return __uint_as_float(s[0]) + __uint_as_float(s[1]);
}
__device__ __forceinline__ float swap32_add(float a, float b) {
  auto s = __builtin_amdgcn_permlane32_swap(__float_as_uint(a), __float_as_uint(b), false, false);
  return __uint_as_float(s[0]) + __uint_as_float(s[1]);
}
__device__ __forceinline__ float swap16_max(float a) {
  auto s = __builtin_amdgcn_permlane16_swap(__float_as_uint(a), __float_as_uint(a), false, false);
  return fmaxf(__uint_as_float(s[0]), __uint_as_float(s[1]));
}
__device__ __forceinline__ float swap32_max(float a) {
  auto s = __builtin_amdgcn_permlane32_swap(__float_as_uint(a), __float_as_uint(a), false, false);
  return fmaxf(__uint_as_float(s[0]), __uint_as_float(s[1]));
}
__device__ __forceinline__ float x16_32_sum(float x) { x = swap16_add(x, x); return swap32_add(x, x); }
__device__ __forceinline__ float x16_32_max(float x) { return swap32_max(swap16_max(x)); }
__device__ __forceinline__ float row16_sum(float x) {
  x += dpp<XOR1>(x); x += dpp<XOR2>(x); x += dpp<HMIRROR>(x); x += dpp<MIRROR>(x);
  return x;
}
__device__ __forceinline__ float wave_sum(float x) { return x16_32_sum(row16_sum(x)); }

__device__ __forceinline__ v4u ldnt(const void* p) { return __builtin_nontemporal_load((const v4u*)p); }
__device__ __forceinline__ f32x4 mfma32(v4u a, v4u b, f32x4 c) {
  return __builtin_amdgcn_mfma_f32_16x16x32_bf16(__builtin_bit_cast(bf16x8, a), __builtin_bit_cast(bf16x8, b), c, 0, 0, 0);
}
__device__ __forceinline__ f32x4 mfma16(v2u a, v2u b, f32x4 c) {
  return __builtin_amdgcn_mfma_f32_16x16x16bf16_1k(__builtin_bit_cast(s16x4, a), __builtin_bit_cast(s16x4, b), c, 0, 0, 0);
}
#define SB() __builtin_amdgcn_sched_barrier(0)
#define BAR() asm volatile("s_waitcnt lgkmcnt(0)\n\ts_barrier" ::: "memory")

__device__ __forceinline__ void slice_reduce(const float (&acc)[16], float* red, int w, int lane) {
  float a8[8], a4[4];
#pragma unroll
  for (int j = 0; j < 8; ++j) a8[j] = swap32_add(acc[j], acc[8 + j]);
#pragma unroll
  for (int j = 0; j < 4; ++j) a4[j] = swap16_add(a8[j], a8[4 + j]);
#pragma unroll
  for (int j = 0; j < 4; ++j) a4[j] += dpp<ROR8>(a4[j]);
  if ((lane & 8) == 0) {
    const int b = lane >> 5, r1 = (lane >> 4) & 1, piece = lane & 7;
    *reinterpret_cast<f32x4*>(red + w * 128 + b * 64 + piece * 8 + r1 * 4) = f32x4{a4[0], a4[1], a4[2], a4[3]};
  }
}

__global__ void __launch_bounds__(NTHR, 2)
fused_decode(const unsigned short* __restrict__ x, const unsigned short* __restrict__ kc, const unsigned short* __restrict__ vc,
             const unsigned short* __restrict__ wq, const unsigned short* __restrict__ wo, const unsigned short* __restrict__ w1,
             const unsigned short* __restrict__ w2, unsigned short* __restrict__ out, unsigned char* ws) {
  __shared__ __attribute__((aligned(16))) unsigned char lds[L_BYTES];
  const int tid = threadIdx.x, lane = tid & 63, w = __builtin_amdgcn_readfirstlane(tid >> 6);
  const int g = blockIdx.x, xc = g & 7, gi = g >> 3;
  const __amdgpu_buffer_rsrc_t rs = __builtin_amdgcn_make_buffer_rsrc(ws, 0, WS_END, 0x00020000);
  const int ab = xc >> 2, kvh = xc & 3, split = gi;
  const size_t kvbase = ((size_t)(ab * 4 + kvh) * SK + (size_t)split * 1024 + 32 * w) * 128;
  const int r16 = lane & 15, gq = lane >> 4;
  const unsigned short* kp = kc + kvbase + (4 * (r16 & 3) + (r16 >> 2)) * 128 + gq * 8;
  const unsigned short* vp = vc + kvbase + lane * 8;
  v4u kr[4][8], vr[4][8];
#define LD_K(c, i_) kr[c][i_] = ldnt(kp + (c) * 256 * 128 + ((i_) >> 2) * 16 * 128 + ((i_) & 3) * 32)
#define ISSUE_K(c) do { _Pragma("unroll") for (int i_ = 0; i_ < 8; ++i_) LD_K(c, i_); SB(); } while (0)
#define ISSUE_V(c) do { _Pragma("unroll") for (int i_ = 0; i_ < 8; ++i_) vr[c][i_] = ldnt(vp + (c) * 256 * 128 + i_ * 512); SB(); } while (0)

  const unsigned epoch = __hip_atomic_load((const unsigned*)(ws + WS_EPOCH), __ATOMIC_RELAXED, __HIP_MEMORY_SCOPE_AGENT);
  unsigned tag;
  {
    const bool is_q = g < 128, has_a = g < 240;
    const int u = is_q ? g : g - 128;
    const int ar = u & 1, ac = u >> 1;
    const int ld = is_q ? NQ : FF;
    const __amdgpu_buffer_rsrc_t rw = __builtin_amdgcn_make_buffer_rsrc((void*)(is_q ? wq : w1), 0, has_a ? DM * ld * 2 : 0, 0x00020000);
    v4u xs[4];
#pragma unroll
    for (int i = 0; i < 4; ++i) xs[i] = *reinterpret_cast<const v4u*>(x + (lane + 64 * i) * 8);
    const int xrow = 512 * ar + 64 * w + lane;
    const unsigned short xr0 = x[xrow], xr1 = x[DM + xrow];
    SB();
    v4u wr[8];
    {
      int off = ((512 * ar + 64 * w + (lane >> 3)) * ld + 64 * ac + 8 * (lane & 7)) * 2;
#pragma unroll
      for (int i = 0; i < 8; ++i) { wr[i] = __builtin_amdgcn_raw_buffer_load_b128(rw, off, 0, AUX_NT); off += 16 * ld; }
    }
    SB();
    float s0 = 0.f, s1 = 0.f;
#pragma unroll
    for (int i = 0; i < 2; ++i)
#pragma unroll
      for (int k = 0; k < 4; ++k) { s0 += bf_lo(xs[i][k]) + bf_hi(xs[i][k]); s1 += bf_lo(xs[2 + i][k]) + bf_hi(xs[2 + i][k]); }
    const float mean0 = wave_sum(s0) * (1.f / DM), mean1 = wave_sum(s1) * (1.f / DM);
    float v0 = 0.f, v1 = 0.f;
#pragma unroll
    for (int i = 0; i < 2; ++i)
#pragma unroll
      for (int k = 0; k < 4; ++k) {
        float d;
        d = bf_lo(xs[i][k]) - mean0; v0 = fmaf(d, d, v0);
        d = bf_hi(xs[i][k]) - mean0; v0 = fmaf(d, d, v0);
        d = bf_lo(xs[2 + i][k]) - mean1; v1 = fmaf(d, d, v1);
        d = bf_hi(xs[2 + i][k]) - mean1; v1 = fmaf(d, d, v1);
      }
    const float var0 = rbf(wave_sum(v0) * (1.f / DM)), var1 = rbf(wave_sum(v1) * (1.f / DM));
    const float m0 = rbf(mean0), m1 = rbf(mean1);
    const float sd0 = rbf(sqrtf(rbf(var0 + 1e-5f))), sd1 = rbf(sqrtf(rbf(var1 + 1e-5f)));
    const unsigned xnp = cvt_pk(rbf(bf_lo(xr0) - m0) / sd0, rbf(bf_lo(xr1) - m1) / sd1);
    unsigned xb[8];
#pragma unroll
    for (int i = 0; i < 8; ++i) xb[i] = __builtin_amdgcn_ds_bpermute((8 * i + (lane >> 3)) * 4, xnp);
    tag = __builtin_amdgcn_readfirstlane(epoch) * 1664525u + 1013904223u;
    SB();
    float acc[16];
#pragma unroll
    for (int j = 0; j < 16; ++j) acc[j] = 0.f;
#pragma unroll
    for (int i = 0; i < 8; ++i) {
      const float x0 = bf_lo(xb[i]), x1 = bf_hi(xb[i]);
#pragma unroll
      for (int k = 0; k < 4; ++k) {
        const float lo = bf_lo(wr[i][k]), hi = bf_hi(wr[i][k]);
        acc[2 * k] = fmaf(x0, lo, acc[2 * k]);
        acc[2 * k + 1] = fmaf(x0, hi, acc[2 * k + 1]);
        acc[8 + 2 * k] = fmaf(x1, lo, acc[8 + 2 * k]);
        acc[8 + 2 * k + 1] = fmaf(x1, hi, acc[8 + 2 * k + 1]);
      }
      SB();
      LD_K(0, i);
      SB();
    }
    float* rq = reinterpret_cast<float*>(lds + L_RQ);
    slice_reduce(acc, rq, w, lane);
    BAR();
    if (w == 0 && has_a) {
      float p0 = 0.f, p1 = 0.f;
#pragma unroll
      for (int ww = 0; ww < 8; ++ww) {
        const float2 v = *reinterpret_cast<const float2*>(rq + ww * 128 + 2 * lane);
        p0 += v.x; p1 += v.y;
      }
      const unsigned off = (is_q ? WS_QG : WS_SG) + (((lane >> 5) * 2 + ar) * ld + 64 * ac + 2 * (lane & 31)) * 8;
      v4u gr = {__float_as_uint(p0), tag, __float_as_uint(p1), tag};
      __builtin_amdgcn_raw_buffer_store_b128(gr, rs, off, 0, AUX_SC1);
    }
    SB();
  }
  ISSUE_V(0);
  ISSUE_K(1);
  const unsigned qoff = WS_QG + (ab * 2 * NQ + kvh * 1024 + 2 * tid) * 8;
  v4u qg[2];
#pragma unroll
  for (int r = 0; r < 2; ++r) qg[r] = __builtin_amdgcn_raw_buffer_load_b128(rs, qoff + r * NQ * 8, 0, AUX_SC1);
  SB();
  asm volatile("s_barrier" ::: "memory");
  SB();
  ISSUE_V(1);
#define QG_OK() (qg[0][1] == tag && qg[0][3] == tag && qg[1][1] == tag && qg[1][3] == tag)
  if (!__all(QG_OK())) {
    for (int spin = 0; spin < SPIN_CAP; ++spin) {
      asm volatile("" ::: "memory");
#pragma unroll
      for (int r = 0; r < 2; ++r) qg[r] = __builtin_amdgcn_raw_buffer_load_b128(rs, qoff + r * NQ * 8, 0, AUX_SC1);
      if (__all(QG_OK())) break;
      __builtin_amdgcn_s_sleep(2);
    }
  }
  reinterpret_cast<unsigned*>(lds + L_Q)[tid] =
      cvt_pk(__uint_as_float(qg[0][0]) + __uint_as_float(qg[1][0]), __uint_as_float(qg[0][2]) + __uint_as_float(qg[1][2]));
  BAR();
  v4u qf[4];
#pragma unroll
  for (int s = 0; s < 4; ++s) qf[s] = *reinterpret_cast<const v4u*>(lds + L_Q + (r16 & 7) * 256 + s * 64 + gq * 16);

  float m_run = -INFINITY, l_run = 0.f;
  f32x4 oacc[4];
#pragma unroll
  for (int a = 0; a < 4; ++a) oacc[a] = f32x4{0.f, 0.f, 0.f, 0.f};
  unsigned pk[8];
  const unsigned pmask = (lane & 8) ? 0xffff0000u : 0x0000ffffu;

#define QK(c) do { \
    f32x4 sc_[2]; \
    _Pragma("unroll") for (int t_ = 0; t_ < 2; ++t_) { \
      sc_[t_] = f32x4{0.f, 0.f, 0.f, 0.f}; \
      _Pragma("unroll") for (int s_ = 0; s_ < 4; ++s_) sc_[t_] = mfma32(kr[c][4 * t_ + s_], qf[s_], sc_[t_]); \
    } \
    float sv_[8]; float mx_ = -INFINITY; \
    _Pragma("unroll") for (int i_ = 0; i_ < 8; ++i_) { sv_[i_] = rbf(rbf(sc_[i_ >> 2][i_ & 3]) * QK_SCALE); mx_ = fmaxf(mx_, sv_[i_]); } \
    mx_ = x16_32_max(mx_); \
    const float mn_ = fmaxf(m_run, mx_); \
    const float al_ = __builtin_amdgcn_exp2f((m_run - mn_) * LOG2E); \
    m_run = mn_; \
    float ps_ = 0.f; \
    _Pragma("unroll") for (int i_ = 0; i_ < 8; ++i_) { \
      const float p_ = __builtin_amdgcn_exp2f((sv_[i_] - mn_) * LOG2E); ps_ += p_; pk[i_] = cvt_pk(p_, p_) & pmask; } \
    l_run = fmaf(l_run, al_, ps_); \
    _Pragma("unroll") for (int a_ = 0; a_ < 4; ++a_) oacc[a_] *= al_; \
  } while (0)
#define PV(c) do { \
    _Pragma("unroll") for (int i_ = 0; i_ < 8; ++i_) { \
      const v2u lo_ = {vr[c][i_][0], vr[c][i_][1]}, hi_ = {vr[c][i_][2], vr[c][i_][3]}; \
      const v2u b0_ = {pk[i_], 0u}, b1_ = {0u, pk[i_]}; \
      oacc[0] = mfma16(lo_, b0_, oacc[0]); oacc[1] = mfma16(lo_, b1_, oacc[1]); \
      oacc[2] = mfma16(hi_, b0_, oacc[2]); oacc[3] = mfma16(hi_, b1_, oacc[3]); \
    } \
  } while (0)

  const bool isP = gi & 1;
  const int idx = (gi >> 1) * 8 + xc;
  constexpr unsigned OOR = 0x7ffffff0u;
  v4u tw[8];

  QK(0); SB(); ISSUE_K(2);
  PV(0); SB(); ISSUE_V(2);
  QK(1); SB(); ISSUE_K(3);
  PV(1); SB(); ISSUE_V(3);
  QK(2); SB();
  PV(2); SB();
  QK(3); SB();
  asm volatile("s_barrier" ::: "memory");
  SB();
  {
    const __amdgpu_buffer_rsrc_t rwo = __builtin_amdgcn_make_buffer_rsrc((void*)wo, 0, isP ? NQ * DM * 2 : 0, 0x00020000);
    const int off = ((128 * (idx >> 2) + 16 * w + gq) * DM + 256 * (idx & 3) + 8 * r16) * 2;
#pragma unroll
    for (int i = 0; i < 8; ++i) tw[i] = __builtin_amdgcn_raw_buffer_load_b128(rwo, off + (i >> 1) * 4 * DM * 2 + (i & 1) * 256, 0, AUX_NT);
    SB();
  }
  PV(3); SB();
  {
    l_run = x16_32_sum(l_run);
    float* orw = reinterpret_cast<float*>(lds + L_OR) + w * OR_W + gq * 272 + r16 * 16;
#pragma unroll
    for (int a = 0; a < 4; ++a) *reinterpret_cast<f32x4*>(orw + 4 * a) = oacc[a];
    if (lane < 8) *reinterpret_cast<float2*>(lds + L_ML + (lane * 8 + w) * 8) = make_float2(m_run, l_run);
  }
  BAR();
  {
    f32x4 ml[4];
#pragma unroll
    for (int i = 0; i < 4; ++i) ml[i] = *reinterpret_cast<const f32x4*>(lds + L_ML + w * 64 + i * 16);
    const float* orr = reinterpret_cast<const float*>(lds + L_OR) + (lane >> 4) * 272 + w * 16 + (lane & 3) * 4 + ((lane >> 2) & 3);
    float oa[8], ob[8];
#pragma unroll
    for (int ww = 0; ww < 8; ++ww) { oa[ww] = orr[ww * OR_W]; ob[ww] = orr[ww * OR_W + 128]; }
    float M = fmaxf(fmaxf(fmaxf(ml[0][0], ml[0][2]), fmaxf(ml[1][0], ml[1][2])), fmaxf(fmaxf(ml[2][0], ml[2][2]), fmaxf(ml[3][0], ml[3][2])));
    float L = 0.f, o0 = 0.f, o1 = 0.f;
#pragma unroll
    for (int ww = 0; ww < 8; ++ww) {
      const float e = __builtin_amdgcn_exp2f((ml[ww >> 1][(ww & 1) * 2] - M) * LOG2E);
      L = fmaf(e, ml[ww >> 1][(ww & 1) * 2 + 1], L);
      o0 = fmaf(e, oa[ww], o0);
      o1 = fmaf(e, ob[ww], o1);
    }
    const unsigned slot = (unsigned)((ab * 32 + kvh * 8 + w) * 32 + split);
    v2u gr = {cvt_pk(o0, o1), tag};
    __builtin_amdgcn_raw_buffer_store_b64(gr, rs, WS_PO + slot * 512 + lane * 8, 0, AUX_SC1);
    if (lane == 0) {
      v4u g2 = {__float_as_uint(M), tag, __float_as_uint(L), tag};
      __builtin_amdgcn_raw_buffer_store_b128(g2, rs, WS_PML + slot * 16, 0, AUX_SC1);
    }
  }
  unsigned mk[4];
#pragma unroll
  for (int r = 0; r < 4; ++r) mk[r] = (r == ((r16 & 7) >> 1)) ? ((r16 & 1) ? 0xffff0000u : 0x0000ffffu) : 0u;
#define VALID(v) (((v)[1] == tag) & ((v)[3] == tag))
  if (isP) {
    const int h = idx >> 2, cb = idx & 3;
    const unsigned slot0 = ((w >> 2) * 32 + h) * 32 + 8 * (w & 3);
    const unsigned pml = lane < 8 ? WS_PML + (slot0 + lane) * 16 : OOR;
    const unsigned po = WS_PO + (slot0 + (lane >> 5)) * 512 + (lane & 31) * 16;
    v4u ml = {0u, ~tag, 0u, ~tag}, og[4] = {ml, ml, ml, ml};
    v4u p_m, p_o[4] = {ml, ml, ml, ml};
    p_m = __builtin_amdgcn_raw_buffer_load_b128(rs, pml, 0, AUX_SC1);
    for (int spin = 0; spin < SPIN_CAP; ++spin) {
      if (VALID(p_m)) ml = p_m;
      bool all = true;
#pragma unroll
      for (int i = 0; i < 4; ++i) { if (VALID(p_o[i])) og[i] = p_o[i]; all &= VALID(og[i]); }
      const unsigned rdy = (unsigned)__ballot(VALID(ml)) & 0xffu;
      if (rdy == 0xffu && __all(all)) break;
      asm volatile("" ::: "memory");
      const unsigned lm = (__popc(rdy) >= 6 ? 0xffu : rdy) >> (lane >> 5);
      p_m = __builtin_amdgcn_raw_buffer_load_b128(rs, VALID(ml) ? OOR : pml, 0, AUX_SC1);
#pragma unroll
      for (int i = 0; i < 4; ++i)
        p_o[i] = __builtin_amdgcn_raw_buffer_load_b128(rs, (!VALID(og[i]) & (bool)((lm >> (2 * i)) & 1u)) ? po + i * 1024 : OOR, 0, AUX_SC1);
    }
    {
      const float ms = __uint_as_float(ml[0]), ls = __uint_as_float(ml[2]);
      float M = fmaxf(ms, dpp<XOR1>(ms)); M = fmaxf(M, dpp<XOR2>(M)); M = fmaxf(M, dpp<HMIRROR>(M));
      const float e = __builtin_amdgcn_exp2f((ms - M) * LOG2E);
      float L = e * ls; L += dpp<XOR1>(L); L += dpp<XOR2>(L); L += dpp<HMIRROR>(L);
      float v0 = 0.f, v1 = 0.f, v2 = 0.f, v3 = 0.f;
#pragma unroll
      for (int i = 0; i < 4; ++i) {
        const float we = __int_as_float(__builtin_amdgcn_ds_bpermute((2 * i + (lane >> 5)) * 4, __float_as_int(e)));
        v0 = fmaf(we, bf_lo(og[i][0]), v0); v1 = fmaf(we, bf_hi(og[i][0]), v1);
        v2 = fmaf(we, bf_lo(og[i][2]), v2); v3 = fmaf(we, bf_hi(og[i][2]), v3);
      }
      const float t0 = swap32_add(v0, v2), t1 = swap32_add(v1, v3);
      float* oc = reinterpret_cast<float*>(lds + L_OC) + ((w >> 2) * 128 + 4 * (lane & 31) + 2 * (lane >> 5)) * 4 + (w & 3);
      oc[0] = t0; oc[4] = t1;
      if (lane == 0) *reinterpret_cast<float2*>(lds + L_WG + w * 8) = make_float2(M, L);
    }
    BAR();
    f32x4 acc[2] = {f32x4{0.f, 0.f, 0.f, 0.f}, f32x4{0.f, 0.f, 0.f, 0.f}};
    {
      const f32x4 a = *reinterpret_cast<const f32x4*>(lds + L_WG + (r16 >> 3) * 32), b = *reinterpret_cast<const f32x4*>(lds + L_WG + (r16 >> 3) * 32 + 16);
      const float M = fmaxf(fmaxf(a[0], a[2]), fmaxf(b[0], b[2]));
      float s0 = __builtin_amdgcn_exp2f((a[0] - M) * LOG2E), s1 = __builtin_amdgcn_exp2f((a[2] - M) * LOG2E);
      float s2 = __builtin_amdgcn_exp2f((b[0] - M) * LOG2E), s3 = __builtin_amdgcn_exp2f((b[2] - M) * LOG2E);
      const float inv = __builtin_amdgcn_rcpf(fmaf(s0, a[1], fmaf(s1, a[3], fmaf(s2, b[1], s3 * b[3]))));
      s0 *= inv; s1 *= inv; s2 *= inv; s3 *= inv;
#pragma unroll
      for (int j = 0; j < 4; ++j) {
        const f32x4 o4 = *reinterpret_cast<const f32x4*>(lds + L_OC + ((r16 >> 3) * 128 + 16 * w + 4 * j + gq) * 16);
        const float o = fmaf(s0, o4[0], fmaf(s1, o4[1], fmaf(s2, o4[2], s3 * o4[3])));
        const unsigned pkv = cvt_pk(o, o);
        const v4u bf = {pkv & mk[0], pkv & mk[1], pkv & mk[2], pkv & mk[3]};
        acc[0] = mfma32(tw[2 * j], bf, acc[0]);
        acc[1] = mfma32(tw[2 * j + 1], bf, acc[1]);
      }
    }
    *reinterpret_cast<f32x4*>(lds + L_RED + (w * 128 + lane) * 16) = acc[0];
    *reinterpret_cast<f32x4*>(lds + L_RED + (w * 128 + 64 + lane) * 16) = acc[1];
    BAR();
    if (tid < 256) {
      const int b = tid >> 7, n = (tid & 127) * 2, c = (n & 127) >> 3;
      const float* red = reinterpret_cast<const float*>(lds + L_RED) + ((n >> 7) * 64 + (c >> 2) * 16 + b * 8 + (n & 7)) * 4 + (c & 3);
      float v0 = 0.f, v1 = 0.f;
#pragma unroll
      for (int ww = 0; ww < 8; ++ww) { v0 += red[ww * 512]; v1 += red[ww * 512 + 4]; }
      v4u gr = {__float_as_uint(v0), tag, __float_as_uint(v1), tag};
      __builtin_amdgcn_raw_buffer_store_b128(gr, rs, WS_P2 + ((h * 2 + b) * 1024 + 256 * cb + n) * 8, 0, AUX_SC1);
    }
  } else {
    const int rb = idx >> 3, c8 = idx & 7;
    const unsigned hoff = (lane & 31) < 28 ? WS_SG + ((lane >> 5) * FF + 224 * rb + 28 * w + (lane & 31)) * 8 : OOR;
    v2u hg[2];
    hg[0] = __builtin_amdgcn_raw_buffer_load_b64(rs, hoff, 0, AUX_SC1);
    hg[1] = __builtin_amdgcn_raw_buffer_load_b64(rs, hoff, 2 * FF * 8, AUX_SC1);
    v4u w2r[7];
    {
      const unsigned short* p = w2 + (size_t)(224 * rb + 28 * w + gq) * DM + 128 * c8 + 8 * r16;
#pragma unroll
      for (int j = 0; j < 7; ++j) w2r[j] = ldnt(p + (size_t)4 * j * DM);
    }
    const float xres = bf_lo(x[(idx & 1) * DM + (idx >> 1) * 16 + 2 * (lane & 7) + (lane >> 5)]);
    SB();
    const bool hskip = (lane & 31) >= 28;
    if (!__all(hskip | ((hg[0][1] == tag) & (hg[1][1] == tag)))) {
      for (int spin = 0; spin < SPIN_CAP; ++spin) {
        asm volatile("" ::: "memory");
        hg[0] = __builtin_amdgcn_raw_buffer_load_b64(rs, hoff, 0, AUX_SC1);
        hg[1] = __builtin_amdgcn_raw_buffer_load_b64(rs, hoff, 2 * FF * 8, AUX_SC1);
        if (__all(hskip | ((hg[0][1] == tag) & (hg[1][1] == tag)))) break;
        __builtin_amdgcn_s_sleep(1);
      }
    }
    const float h0 = rbf(swap32_add(__uint_as_float(hg[0][0]), __uint_as_float(hg[0][0]))), h1 = rbf(swap32_add(__uint_as_float(hg[1][0]), __uint_as_float(hg[1][0])));
    const int spk = (int)cvt_pk(h0 / (1.f + __expf(-h0)), h1 / (1.f + __expf(-h1)));
    f32x4 acc = {0.f, 0.f, 0.f, 0.f};
#pragma unroll
    for (int j = 0; j < 7; ++j) {
      const unsigned sp = (unsigned)__builtin_amdgcn_ds_bpermute((4 * j + gq) * 4, spk);
      const unsigned v = (r16 & 8) ? (sp >> 16) : (sp & 0xffffu), pkv = v | (v << 16);
      const v4u bf = {pkv & mk[0], pkv & mk[1], pkv & mk[2], pkv & mk[3]};
      acc = mfma32(w2r[j], bf, acc);
    }
    *reinterpret_cast<f32x4*>(lds + L_RED + (w * 64 + lane) * 16) = acc;
    BAR();
    if (tid < 128) {
      const int b = tid >> 6, n = (tid & 63) * 2, c = n >> 3;
      const float* red = reinterpret_cast<const float*>(lds + L_RED) + ((c >> 2) * 16 + b * 8 + (n & 7)) * 4 + (c & 3);
      float v0 = 0.f, v1 = 0.f;
#pragma unroll
      for (int ww = 0; ww < 8; ++ww) { v0 += red[ww * 256]; v1 += red[ww * 256 + 4]; }
      v4u gr = {__float_as_uint(v0), tag, __float_as_uint(v1), tag};
      __builtin_amdgcn_raw_buffer_store_b128(gr, rs, WS_P2 + (((32 + rb) * 2 + b) * 1024 + 128 * c8 + n) * 8, 0, AUX_SC1);
    }
    if (w == 0) {
      const int b = idx & 1, c0 = (idx >> 1) * 16;
      const unsigned off = WS_P2 + (((lane >> 3) * 2 + b) * 1024 + c0) * 8 + (lane & 7) * 16;
      v4u pg[6];
      for (int spin = 0; spin < SPIN_CAP; ++spin) {
        asm volatile("" ::: "memory");
        const v4u gt = __builtin_amdgcn_raw_buffer_load_b128(rs, lane < 32 ? WS_P2 + ((lane >> 3) * 16 + b) * 8192 + c0 * 8 + (lane & 7) * 16 : OOR, 0, AUX_SC1);
        if (__any(VALID(gt))) break;
      }
#pragma unroll
      for (int i = 0; i < 6; ++i) pg[i] = __builtin_amdgcn_raw_buffer_load_b128(rs, off + i * (8 * 2 * 1024 * 8), 0, AUX_SC1);
      for (int spin = 0; spin < SPIN_CAP; ++spin) {
        bool ok = true;
#pragma unroll
        for (int i = 0; i < 6; ++i) ok &= VALID(pg[i]);
        if (__all(ok)) break;
        asm volatile("" ::: "memory");
#pragma unroll
        for (int i = 0; i < 6; ++i) {
          const bool vi = VALID(pg[i]);
          const v4u nw = __builtin_amdgcn_raw_buffer_load_b128(rs, vi ? OOR : off + i * (8 * 2 * 1024 * 8), 0, AUX_SC1);
          if (!vi) pg[i] = nw;
        }
      }
      float v0 = 0.f, v1 = 0.f;
#pragma unroll
      for (int i = 0; i < 6; ++i) { v0 += __uint_as_float(pg[i][0]); v1 += __uint_as_float(pg[i][2]); }
      float r = swap32_add(v0, v1);
      r = swap16_add(r, r);
      r += dpp<ROR8>(r);
      if ((lane & 24) == 0) out[b * DM + c0 + 2 * (lane & 7) + (lane >> 5)] = (unsigned short)(cvt_pk(xres + r, 0.f) & 0xffffu);
      if (g == 0 && lane == 0) __hip_atomic_store((unsigned*)(ws + WS_EPOCH), tag, __ATOMIC_RELAXED, __HIP_MEMORY_SCOPE_AGENT);
    }
  }
}

}

extern "C" void kernel_launch(void* const* d_in, const int* in_sizes, int n_in, void* d_out, int out_size, void* d_ws, size_t ws_size,
                              hipStream_t stream) {
  (void)in_sizes; (void)n_in; (void)out_size; (void)ws_size;
  hipLaunchKernelGGL(fused_decode, dim3(NWG), dim3(NTHR), 0, stream, (const unsigned short*)d_in[0], (const unsigned short*)d_in[1],
                     (const unsigned short*)d_in[2], (const unsigned short*)d_in[3], (const unsigned short*)d_in[4],
                     (const unsigned short*)d_in[6], (const unsigned short*)d_in[7], (unsigned short*)d_out, (unsigned char*)d_ws);
}
```
